```python
import torch

def _softmax(x, axis=-1):
    return torch.softmax(x, dim=axis)

def _gelu(x):
    return torch.nn.functional.gelu(x, approximate='tanh')

def _silu(x):
    return torch.nn.functional.silu(x)

def _sigmoid(x):
    return torch.sigmoid(x)

def _layernorm(x, axis=-1, eps=1e-05):
    m = x.mean(dim=axis, keepdim=True)
    v = x.var(dim=axis, keepdim=True, unbiased=False)
    return (x - m) / torch.sqrt(v + eps)

def _scatter_add(tgt, idx, src, axis=0):
    return tgt.clone().index_add_(axis, idx, src)

def _segment_sum(src, seg, n):
    out = torch.zeros((n,) + src.shape[1:], dtype=src.dtype)
    return out.index_add_(0, seg, src)

def _pad(x, pw):
    flat = tuple((v for pair in reversed(pw) for v in pair))
    return torch.nn.functional.pad(x, flat)
kernel_name = 'adaLN_cond__sliding_block_cached__glu_gated_swiglu__decode'

def setup_inputs():

    _w = lambda a: (lambda t: t.to(torch.bfloat16) if t.is_floating_point() else t)(torch.as_tensor(a))

    def _A_setup():
        B, Sq, D = (8, 1, 448)
        Hq, Hkv, Sk, Dh = (16, 8, 4096, 256)
        assert Hq % Hkv == 0, (Hq, Hkv)
        return {'x': _w((lambda *s: torch.randn(*s, dtype=torch.bfloat16) * 0.1)(B, Sq, D)), 'k_cache': _w((lambda *s: torch.randn(*s, dtype=torch.bfloat16) * 0.8)(B, Hkv, Sk, Dh)), 'v_cache': _w((lambda *s: torch.randn(*s, dtype=torch.bfloat16) * 3.2)(B, Hkv, Sk, Dh)), 'wq': _w((lambda *s: torch.randn(*s, dtype=torch.bfloat16) * 0.8)(D, Hq * Dh) * D ** (-0.5)), 'wo': _w((lambda *s: torch.randn(*s, dtype=torch.bfloat16) * 3.2)(Hq * Dh, D) * (Hq * Dh) ** (-0.5))}
    _A_in = _A_setup()

    def _B_setup():
        B, S, D, F = (8, 1, 448, 1792)
        return {'x': _w((lambda *s: torch.randn(*s, dtype=torch.bfloat16) * 0.1)(B, S, D)), 'w1': _w((lambda *s: torch.randn(*s, dtype=torch.bfloat16) * 3.2)(D, F) * (2.0 / D) ** 0.5), 'w3': _w((lambda *s: torch.randn(*s, dtype=torch.bfloat16) * 3.2)(D, F) * (2.0 / D) ** 0.5), 'w2': _w((lambda *s: torch.randn(*s, dtype=torch.bfloat16) * 3.2)(F, D) * (2.0 / F) ** 0.5)}
    _B_in = _B_setup()
    return {'A_x': _A_in['x'], 'A_k_cache': _A_in['k_cache'], 'A_v_cache': _A_in['v_cache'], 'A_wq': _A_in['wq'], 'A_wo': _A_in['wo'], 'B_x': _B_in['x'], 'B_w1': _B_in['w1'], 'B_w3': _B_in['w3'], 'B_w2': _B_in['w2'], 'c': _w((lambda *s: torch.randn(*s, dtype=torch.bfloat16) * 0.8)(8, 448) * 8.0), 'w_mod': _w((lambda *s: torch.randn(*s, dtype=torch.bfloat16) * 0.8)(448, 6 * 448) * 448 ** (-0.5) * 8.0)}

def reference(A_x, A_k_cache, A_v_cache, A_wq, A_wo, B_x, B_w1, B_w3, B_w2, c, w_mod):

    def _A_ref(x, k_cache, v_cache, wq, wo):
        B, Sq, D = x.shape
        Hq, Dh = (16, 256)
        Hkv, Sk = (k_cache.shape[1], k_cache.shape[2])
        W = min(4096, max(256, Sk // 4))
        rep = Hq // Hkv
        q = (lambda x, *d: x.permute(*d))((x @ wq).reshape(B, Sq, Hq, Dh), 0, 2, 1, 3)
        k = (lambda x, n, axis: torch.repeat_interleave(x, n, dim=axis))(k_cache[:, :, Sk - W:, :], rep, axis=1)
        v = (lambda x, n, axis: torch.repeat_interleave(x, n, dim=axis))(v_cache[:, :, Sk - W:, :], rep, axis=1)
        scores = torch.einsum('bhqd,bhkd->bhqk', q, k) * Dh ** (-0.5)
        p = _softmax(scores, axis=-1)
        o = torch.einsum('bhqk,bhkd->bhqd', p, v)
        return (lambda x, *d: x.permute(*d))(o, 0, 2, 1, 3).reshape(B, Sq, Hq * Dh) @ wo

    def _B_ref(x, w1, w3, w2):
        gate = torch.einsum('bsd,df->bsf', x, w1)
        up = torch.einsum('bsd,df->bsf', x, w3)
        h = _silu(gate) * up
        out = torch.einsum('bsf,fd->bsd', h, w2)
        return out
    x = A_x
    mod = (_silu(c) @ w_mod).reshape(c.shape[0], 6, 1, -1)
    sa, ca, ga, sb, cb, gb = (mod[:, 0].clone(), mod[:, 1].clone(), mod[:, 2].clone(), mod[:, 3].clone(), mod[:, 4].clone(), mod[:, 5].clone())
    h = _layernorm(x, axis=-1) * (1 + ca) + sa
    x = x + ga * _A_ref(h, A_k_cache, A_v_cache, A_wq, A_wo)
    h = _layernorm(x, axis=-1) * (1 + cb) + sb
    x = x + gb * _B_ref(h, B_w1, B_w3, B_w2)
    return x
```

```cpp
#include <hip/hip_runtime.h>
#include <hip/hip_bf16.h>
#include <stdint.h>

#define MIMIC 1

typedef unsigned short u16;
__device__ __forceinline__ float bf2f(u16 b) { return __builtin_bit_cast(float, (unsigned)b << 16); }
__device__ __forceinline__ u16 f2bf(float f) { __bf16 h = (__bf16)f; return __builtin_bit_cast(u16, h); }
__device__ __forceinline__ float rb(float f) {
  return bf2f(f2bf(f));
}
constexpr int B = 8, D = 448, HQ = 16, HKV = 8, SK = 4096, DH = 256, W = 1024, F = 1792;

__global__ void k_mod(const u16* c, const u16* wmod, float* mod) {
  __shared__ float sc[B][D];
  for (int i = threadIdx.x; i < B * D; i += blockDim.x) { float v = bf2f(c[i]); sc[i / D][i % D] = rb(v / (1.f + expf(-v))); }
  __syncthreads();
  int n = blockIdx.x * blockDim.x + threadIdx.x;
  if (n >= 6 * D) return;
  float acc[B] = {};
  for (int j = 0; j < D; ++j) { float w = bf2f(wmod[j * 6 * D + n]);
#pragma unroll
    for (int b = 0; b < B; ++b) acc[b] += sc[b][j] * w; }
  for (int b = 0; b < B; ++b) mod[b * 6 * D + n] = rb(acc[b]);
}
__device__ void ln_mod(const float* x  , const float* mod, int si, int ci, float (*h)[D]) {
  int wave = threadIdx.x >> 6, lane = threadIdx.x & 63, nw = blockDim.x >> 6;
  for (int b = wave; b < B; b += nw) {
    float s = 0.f;
    for (int d = lane; d < D; d += 64) s += x[b * D + d];
    for (int o = 32; o; o >>= 1) s += __shfl_xor(s, o);
    float mean = s / D;
    float v = 0.f;
    for (int d = lane; d < D; d += 64) { float t = x[b * D + d] - mean; v += t * t; }
    for (int o = 32; o; o >>= 1) v += __shfl_xor(v, o);
    float m = rb(mean), var = rb(v / D);
    float sd = rb(sqrtf(rb(var + 1e-5f)));
    for (int d = lane; d < D; d += 64) {
      float t = rb(rb(x[b * D + d] - m) / sd);
      float cc = rb(1.f + mod[b * 6 * D + ci * D + d]);
      h[b][d] = rb(rb(t * cc) + mod[b * 6 * D + si * D + d]);
    }
  }
}
__global__ void k_q(const u16* x, const float* mod, const u16* wq, float* q) {
  __shared__ float xs[B * D];
  __shared__ float h[B][D];
  for (int i = threadIdx.x; i < B * D; i += blockDim.x) xs[i] = bf2f(x[i]);
  __syncthreads();
  ln_mod(xs, mod, 0, 1, h);
  __syncthreads();
  int n = blockIdx.x * blockDim.x + threadIdx.x;
  float acc[B] = {};
  for (int j = 0; j < D; ++j) { float w = bf2f(wq[j * HQ * DH + n]);
#pragma unroll
    for (int b = 0; b < B; ++b) acc[b] += h[b][j] * w; }
  for (int b = 0; b < B; ++b) q[b * HQ * DH + n] = rb(acc[b]);
}
__global__ void k_attn(const float* q, const u16* kc, const u16* vc, float* o) {
  __shared__ float qs[DH];
  __shared__ float sc[W];
  __shared__ float red[8];
  int b = blockIdx.x / HQ, h = blockIdx.x % HQ, kvh = h / 2;
  int tid = threadIdx.x, wave = tid >> 6, lane = tid & 63;
  qs[tid] = q[b * HQ * DH + h * DH + tid];
  __syncthreads();
  const u16* kb = kc + ((size_t)(b * HKV + kvh) * SK + (SK - W)) * DH;
  const u16* vb = vc + ((size_t)(b * HKV + kvh) * SK + (SK - W)) * DH;
  for (int k = wave; k < W; k += 4) {
    float s = 0.f;
    for (int d = lane; d < DH; d += 64) s += qs[d] * bf2f(kb[(size_t)k * DH + d]);
    for (int of = 32; of; of >>= 1) s += __shfl_xor(s, of);
    if (lane == 0) sc[k] = rb(rb(s) * 0.0625f);
  }
  __syncthreads();
  float m = -INFINITY;
  for (int k = tid; k < W; k += 256) m = fmaxf(m, sc[k]);
  for (int of = 32; of; of >>= 1) m = fmaxf(m, __shfl_xor(m, of));
  if (lane == 0) red[wave] = m;
  __syncthreads();
  m = fmaxf(fmaxf(red[0], red[1]), fmaxf(red[2], red[3]));
  float l = 0.f;
  for (int k = tid; k < W; k += 256) { float e = expf(sc[k] - m); sc[k] = e; l += e; }
  for (int of = 32; of; of >>= 1) l += __shfl_xor(l, of);
  if (lane == 0) red[4 + wave] = l;
  __syncthreads();
  l = red[4] + red[5] + red[6] + red[7];
  float acc = 0.f;
  for (int k = 0; k < W; ++k) acc += rb(sc[k] / l) * bf2f(vb[(size_t)k * DH + tid]);
  o[b * HQ * DH + h * DH + tid] = rb(acc);
}
__global__ void k_wo(const float* o, const u16* wo, const u16* x, const float* mod, float* x1) {
  __shared__ float os[HQ * DH];
  int b = blockIdx.x, n = threadIdx.x;
  for (int i = n; i < HQ * DH; i += blockDim.x) os[i] = o[b * HQ * DH + i];
  __syncthreads();
  float acc = 0.f;
  for (int j = 0; j < HQ * DH; ++j) acc += os[j] * bf2f(wo[j * D + n]);
  x1[b * D + n] = rb(bf2f(x[b * D + n]) + rb(mod[b * 6 * D + 2 * D + n] * rb(acc)));
}
__global__ void k_up(const float* x1, const float* mod, const u16* w1, const u16* w3, float* hh) {
  __shared__ float xs[B * D];
  __shared__ float h[B][D];
  for (int i = threadIdx.x; i < B * D; i += blockDim.x) xs[i] = x1[i];
  __syncthreads();
  ln_mod(xs, mod, 3, 4, h);
  __syncthreads();
  int n = blockIdx.x * blockDim.x + threadIdx.x;
  float g[B] = {}, u[B] = {};
  for (int j = 0; j < D; ++j) { float a = bf2f(w1[j * F + n]), c = bf2f(w3[j * F + n]);
#pragma unroll
    for (int b = 0; b < B; ++b) { g[b] += h[b][j] * a; u[b] += h[b][j] * c; } }
  for (int b = 0; b < B; ++b) { float gg = rb(g[b]); float s = rb(gg / (1.f + expf(-gg))); hh[b * F + n] = rb(s * rb(u[b])); }
}
__global__ void k_down(const float* hh, const u16* w2, const float* x1, const float* mod, u16* out) {
  __shared__ float hs[F];
  int b = blockIdx.x, n = threadIdx.x;
  for (int i = n; i < F; i += blockDim.x) hs[i] = hh[b * F + i];
  __syncthreads();
  float acc = 0.f;
  for (int j = 0; j < F; ++j) acc += hs[j] * bf2f(w2[j * D + n]);
  out[b * D + n] = f2bf(x1[b * D + n] + rb(mod[b * 6 * D + 5 * D + n] * rb(acc)));
}

extern "C" void kernel_launch(void* const* d_in, const int* in_sizes, int n_in, void* d_out, int out_size, void* d_ws, size_t ws_size, hipStream_t stream) {
  const u16* x = (const u16*)d_in[0]; const u16* kc = (const u16*)d_in[1]; const u16* vc = (const u16*)d_in[2];
  const u16* wq = (const u16*)d_in[3]; const u16* wo = (const u16*)d_in[4];
  const u16* w1 = (const u16*)d_in[6]; const u16* w3 = (const u16*)d_in[7]; const u16* w2 = (const u16*)d_in[8];
  const u16* c = (const u16*)d_in[9]; const u16* wmod = (const u16*)d_in[10];
  float* ws = (float*)d_ws;
  float* mod = ws; float* q = mod + B * 6 * D; float* o = q + B * HQ * DH; float* x1 = o + B * HQ * DH; float* hh = x1 + B * D;
  k_mod<<<42, 64, 0, stream>>>(c, wmod, mod);
  k_q<<<16, 256, 0, stream>>>(x, mod, wq, q);
  k_attn<<<B * HQ, 256, 0, stream>>>(q, kc, vc, o);
  k_wo<<<B, D, 0, stream>>>(o, wo, x, mod, x1);
  k_up<<<7, 256, 0, stream>>>(x1, mod, w1, w3, hh);
  k_down<<<B, D, 0, stream>>>(hh, w2, x1, mod, (u16*)d_out);
}
```

```cpp
#include <hip/hip_runtime.h>
#include <hip/hip_bf16.h>
#include <stdint.h>

#define STAMPS 0
#define KDEPTH 6
#define VDEPTH 6
#define LATE_AT 0

typedef unsigned short u16;
typedef unsigned long long u64;
typedef unsigned v4u __attribute__((ext_vector_type(4)));
typedef unsigned v2u __attribute__((ext_vector_type(2)));
typedef float f32x4 __attribute__((ext_vector_type(4)));
typedef float f32x2 __attribute__((ext_vector_type(2)));
typedef __bf16 bf16x8 __attribute__((ext_vector_type(8)));
typedef __bf16 bf16x2 __attribute__((ext_vector_type(2)));
typedef short s16x4 __attribute__((ext_vector_type(4)));
#define LAS __attribute__((address_space(3)))
#define DEV __device__ __forceinline__
typedef __amdgpu_buffer_rsrc_t rsrc_t;

constexpr int AUX_NT = 2, AUX_SC1 = 16;
constexpr int WS_EPOCH = 0, WS_STATS = 4096, WS_H1 = 8192, WS_H2 = WS_H1 + 8 * 2688 * 4  ;
constexpr int WS_H3 = WS_H2 + 8 * 4096 * 4  , WS_H4 = WS_H3 + 262144, WS_H5 = WS_H4 + 458752, WS_H6 = WS_H5 + 14336;
constexpr int WS_END = WS_H6 + 1605632;
constexpr int CLR_KB = (WS_END - WS_H3) / 1024;
constexpr int WS_CLR_END = WS_H3 + 256 * 9 * 1024;
static_assert(CLR_KB <= 256 * 9 && (WS_END - WS_H3) % 1024 == 0, "clear partition");
constexpr int WS_STAMP = 4 << 20;
constexpr int WS_BYTES = STAMPS ? (8 << 20) : WS_CLR_END;
constexpr int L_EW = 0;
constexpr int L_LW = 28672;
constexpr int L_W2 = L_LW + 28672;
constexpr int L_XN = L_W2 + 14336;
constexpr int L_REDV = L_XN + 7168;
constexpr int L_REDK = L_REDV + 4096;
constexpr int L_STK = L_REDK + 8192;
constexpr int L_Q = L_STK + 512;
constexpr int L_P = L_Q + 1024;
constexpr int L_ML = L_P + 1024;
constexpr int L_FLAG = L_ML + 128;
constexpr int L_BYTES = L_FLAG + 64;
enum { F_XN = 0, F_REDV = 1, F_Q = 2, F_ML = 3, F_GLOB = 4, F_P = 5, F_SHADOW = 6, F_LATE = 7 };

constexpr unsigned SPIN_G = 1u << 16, SPIN_L = 1u << 22;

struct Args { const u16 *x, *kc, *vc, *wq, *wo, *w1, *w3, *w2, *c, *wmod; u16* out; unsigned char* ws; };

DEV rsrc_t mkrsrc(const void* p, unsigned bytes) { return __builtin_amdgcn_make_buffer_rsrc((void*)p, 0, bytes, 0x00020000); }
DEV f32x4 mfma(v4u a, v4u b, f32x4 c) { return __builtin_amdgcn_mfma_f32_16x16x32_bf16(__builtin_bit_cast(bf16x8, a), __builtin_bit_cast(bf16x8, b), c, 0, 0, 0); }
DEV unsigned pk(float lo, float hi) { f32x2 v = {lo, hi}; return __builtin_bit_cast(unsigned, __builtin_convertvector(v, bf16x2)); }
DEV float asf(unsigned u) { return __builtin_bit_cast(float, u); }
DEV unsigned asu(float f) { return __builtin_bit_cast(unsigned, f); }
DEV float lo16(unsigned u) { return asf(u << 16); }
DEV float hi16(unsigned u) { return asf(u & 0xffff0000u); }
DEV float rb(float f) { return lo16(pk(f, f)); }
template <int CTRL> DEV float dpp(float x) { return asf(__builtin_amdgcn_mov_dpp((int)asu(x), CTRL, 0xf, 0xf, true)); }
constexpr int ROR1 = 0x121, ROR2 = 0x122, ROR4 = 0x124, ROR8 = 0x128;
DEV float sum32swap(float a, float b) { auto r = __builtin_amdgcn_permlane32_swap(asu(a), asu(b), false, false); return asf(r[0]) + asf(r[1]); }
DEV float sum16swap(float a, float b) { auto r = __builtin_amdgcn_permlane16_swap(asu(a), asu(b), false, false); return asf(r[0]) + asf(r[1]); }
DEV float max32(float a) { auto r = __builtin_amdgcn_permlane32_swap(asu(a), asu(a), false, false); return fmaxf(asf(r[0]), asf(r[1])); }
DEV float rowsum(float v) { v += dpp<ROR8>(v); v += dpp<ROR4>(v); v += dpp<ROR2>(v); v += dpp<ROR1>(v); return v; }
DEV float rowmax(float v) { v = fmaxf(v, dpp<ROR8>(v)); v = fmaxf(v, dpp<ROR4>(v)); v = fmaxf(v, dpp<ROR2>(v)); v = fmaxf(v, dpp<ROR1>(v)); return v; }
DEV float wavesum(float v) { v = rowsum(v); v = sum16swap(v, v); return sum32swap(v, v); }
DEV unsigned umax3(unsigned a, unsigned b, unsigned c) { return max(max(a, b), c); }
DEV unsigned smax(unsigned acc, v4u v) { return umax3(umax3(acc, v.x, v.y), v.z, v.w); }
DEV unsigned tagx(unsigned acc, v4u v, unsigned tag) { return acc | (v.x ^ tag) | (v.y ^ tag) | (v.z ^ tag) | (v.w ^ tag); }
DEV v4u nosent(v4u v) { v.x = min(v.x, 0xfffffffeu); v.y = min(v.y, 0xfffffffeu); v.z = min(v.z, 0xfffffffeu); v.w = min(v.w, 0xfffffffeu); return v; }
template <int N> DEV void waitvm() { asm volatile("s_waitcnt vmcnt(%0)" ::"i"(N) : "memory"); }
DEV float silu_p(float v) { return v / (1.f + __builtin_amdgcn_exp2f(v * -1.4426950408889634f)); }
DEV float silu(float v) { return v * __builtin_amdgcn_rcpf(1.f + __builtin_amdgcn_exp2f(v * -1.4426950408889634f)); }

DEV v4u ldws(rsrc_t rs, int off) { return __builtin_amdgcn_raw_buffer_load_b128(rs, off, 0, AUX_SC1); }
DEV unsigned ldws1(rsrc_t rs, int off) { return __builtin_amdgcn_raw_buffer_load_b32(rs, off, 0, AUX_SC1); }
DEV void stws(rsrc_t rs, int off, v4u v) { __builtin_amdgcn_raw_buffer_store_b128(v, rs, off, 0, AUX_SC1); }

DEV void lds_signal(LAS unsigned char* L, int flag, int lane) {
  asm volatile("s_waitcnt lgkmcnt(0)" ::: "memory");
  if (lane == 0) __hip_atomic_fetch_add((LAS unsigned*)(L + L_FLAG) + flag, 1u, __ATOMIC_RELAXED, __HIP_MEMORY_SCOPE_WORKGROUP);
}
DEV bool lds_wait(LAS unsigned char* L, int flag, unsigned want) {
  _Pragma("unroll 1") for (unsigned sp = 0;; ++sp) {
      asm volatile("" ::: "memory");
    unsigned v = __builtin_amdgcn_readfirstlane(__hip_atomic_load((LAS unsigned*)(L + L_FLAG) + flag, __ATOMIC_RELAXED, __HIP_MEMORY_SCOPE_WORKGROUP));
    if (v >= want) break;
    if (sp > SPIN_L) return false;
    __builtin_amdgcn_s_sleep(1);
  }
  asm volatile("" ::: "memory");
  return true;
}
DEV v4u trfrag(LAS unsigned char* p0, LAS unsigned char* p1) {
  s16x4 a = __builtin_amdgcn_ds_read_tr16_b64_v4i16((LAS s16x4*)p0), b = __builtin_amdgcn_ds_read_tr16_b64_v4i16((LAS s16x4*)p1);
  v2u x = __builtin_bit_cast(v2u, a), y = __builtin_bit_cast(v2u, b);
  return v4u{x.x, x.y, y.x, y.y};
}
DEV void read_wfrags(LAS unsigned char* img, int wv, int lane, v4u (&wf)[2][2][2]) {
  const int g = lane >> 4, q = (lane >> 2) & 3, p = lane & 3;
#pragma unroll
  for (int pi = 0; pi < 2; ++pi) {
    const int P = (pi == 1 && wv == 3) ? 3 : wv + 4 * pi;
#pragma unroll
    for (int hf = 0; hf < 2; ++hf)
#pragma unroll
      for (int ct = 0; ct < 2; ++ct) {
        LAS unsigned char* b = img + (64 * P + 32 * hf + 8 * g + q) * 64 + ct * 32 + p * 8;
        wf[pi][hf][ct] = trfrag(b, b + 256);
      }
  }
}
DEV f32x4 gemv_partial(const v4u (&wf)[2][2][2], const v4u (&act)[2], int lane) {
  f32x4 z = {0.f, 0.f, 0.f, 0.f};
  f32x4 a0 = mfma(wf[0][0][0], act[0], z), a1 = mfma(wf[0][0][1], act[0], z), b0 = mfma(wf[0][1][0], act[0], z), b1 = mfma(wf[0][1][1], act[0], z);
  a0 = mfma(wf[1][0][0], act[1], a0); a1 = mfma(wf[1][0][1], act[1], a1); b0 = mfma(wf[1][1][0], act[1], b0); b1 = mfma(wf[1][1][1], act[1], b1);
  const bool lo = (lane & 8) == 0;
  f32x4 r;
#pragma unroll
  for (int i = 0; i < 4; ++i) { const float u = lo ? a0[i] : b1[i], v = lo ? a1[i] : b0[i]; r[i] = u + dpp<ROR8>(v); }
  return r;
}
DEV f32x4 red4(LAS unsigned char* red, int lane) {
  LAS f32x4* p = (LAS f32x4*)(red + lane * 16);
  return (p[0] + p[64]) + (p[128] + p[192]);
}
DEV int colchunk(int m, int rho) { return 8 * (m >> 1) + 2 * (((rho & 1) << 1) | (rho >> 1)) + (m & 1); }

#define STAMP(i) do {} while (0)
#define STAMP_OUT() do {} while (0)

__global__ void __launch_bounds__(512, 2) fused_block(Args a) {
  extern __shared__ __attribute__((aligned(16))) unsigned char smem[];
  LAS unsigned char* L = (LAS unsigned char*)smem;
  const int lane = threadIdx.x & 63, w = __builtin_amdgcn_readfirstlane(threadIdx.x >> 6);
  const int bid = blockIdx.x, xc = bid & 7, r = bid >> 3;
  const int kvh = xc, b = r >> 2, s = r & 3;
  STAMP(0);
  const rsrc_t rws = mkrsrc(a.ws, WS_BYTES);
  const size_t kvoff = ((size_t)(b * 8 + kvh) * 4096 + 3072 + s * 256) * 256;
  const int rot = (bid * 5 + (bid >> 3)) & 31;

  if (w < 4) {
    const int vw = w;
    const int e = 2 * ((r >> 1) * 8 + xc) + (r & 1);
    const bool isM = e < 84, isQ = e >= 84 && e < 212;
    const unsigned ep = ldws1(rws, WS_EPOCH);
    if (w == 0 && lane < 16) ((LAS unsigned*)(L + L_FLAG))[lane] = 0u;
    if (isM || isQ) {
      const int col = lane & 15, g = lane >> 4, hb = col & 7, hf = col >> 3;
      const int P1 = vw == 3 ? 3 : vw + 4;
      const int k0 = 64 * vw + 32 * hf + 8 * g, k1 = 64 * P1 + 32 * hf + 8 * g;
      v4u act[2];
      v4u in0, in1;
      if (isM) {
        const rsrc_t rc = mkrsrc(a.c, 8 * 448 * 2);
        in0 = __builtin_amdgcn_raw_buffer_load_b128(rc, (hb * 448 + k0) * 2, 0, 0);
        in1 = __builtin_amdgcn_raw_buffer_load_b128(rc, (hb * 448 + k1) * 2, 0, 0);
      } else {
        const rsrc_t rx = mkrsrc(a.x, 8 * 448 * 2);
        const int o = lane < 56 ? (2 * vw * 448 + lane * 8) * 2 : 0x7fffff00;
        in0 = __builtin_amdgcn_raw_buffer_load_b128(rx, o, 0, 0);
        in1 = __builtin_amdgcn_raw_buffer_load_b128(rx, o, 896, 0);
      }
      {
        const int stride = isM ? 5376 : 8192;
        const rsrc_t rw = isM ? mkrsrc(a.wmod + e * 32, 448 * 5376 - e * 64) : mkrsrc(a.wq + (e - 84) * 32, 448 * 8192 - (e - 84) * 64);
        const int vo = (lane >> 2) * stride + (lane & 3) * 16;
#pragma unroll
        for (int i = 0; i < 4; ++i) __builtin_amdgcn_raw_ptr_buffer_load_lds(rw, (LAS void*)(L + L_EW + (4 * vw + i) * 1024), 16, vo, (4 * vw + i) * 16 * stride, 0, AUX_NT);
        if (vw < 3) {
#pragma unroll
          for (int i = 0; i < 4; ++i) __builtin_amdgcn_raw_ptr_buffer_load_lds(rw, (LAS void*)(L + L_EW + (4 * vw + 16 + i) * 1024), 16, vo, (4 * vw + 16 + i) * 16 * stride, 0, AUX_NT);
        }
      }
      asm volatile("s_waitcnt lgkmcnt(0)" ::: "memory");
      __builtin_amdgcn_s_barrier();
      STAMP(1);
      const unsigned tag = (__builtin_amdgcn_readfirstlane(ep) + 1u) & 0xffffu;
      if (isM) {
#pragma unroll
        for (int i = 0; i < 4; ++i) { act[0][i] = pk(silu_p(lo16(in0[i])), silu_p(hi16(in0[i]))); act[1][i] = pk(silu_p(lo16(in1[i])), silu_p(hi16(in1[i]))); }
      } else {
#pragma unroll
        for (int rr = 0; rr < 2; ++rr) {
          const v4u in = rr ? in1 : in0;
          float v[8];
#pragma unroll
          for (int i = 0; i < 4; ++i) { v[2 * i] = lo16(in[i]); v[2 * i + 1] = hi16(in[i]); }
          float sm = 0.f;
#pragma unroll
          for (int i = 0; i < 8; ++i) sm += v[i];
          const float mean = wavesum(sm) * (1.f / 448.f);
          float sq = 0.f;
#pragma unroll
          for (int i = 0; i < 8; ++i) { const float d = lane < 56 ? v[i] - mean : 0.f; sq += d * d; }
          const float var = rb(wavesum(sq) * (1.f / 448.f)), mb = rb(mean);
          const float sd = rb(sqrtf(rb(var + 1e-5f)));
          v4u o;
#pragma unroll
          for (int i = 0; i < 4; ++i) o[i] = pk(rb(v[2 * i] - mb) / sd, rb(v[2 * i + 1] - mb) / sd);
          if (lane < 56) *(LAS v4u*)(L + L_XN + ((2 * vw + rr) * 448 + lane * 8) * 2) = o;
        }
        lds_signal(L, F_XN, lane);
        if (!lds_wait(L, F_XN, 4)) return;
        const v4u xn0 = *(LAS v4u*)(L + L_XN + (hb * 448 + k0) * 2), xn1 = *(LAS v4u*)(L + L_XN + (hb * 448 + k1) * 2);
        __builtin_amdgcn_s_setprio(2);
        v4u m[8];
        _Pragma("unroll 1") for (unsigned sp = 0;; ++sp) {
      asm volatile("" ::: "memory");
          const int o0 = WS_H1 + (hb * 2688 + k0) * 4, o1 = WS_H1 + (hb * 2688 + k1) * 4;
          m[0] = ldws(rws, o0); m[1] = ldws(rws, o0 + 16); m[2] = ldws(rws, o0 + 1792); m[3] = ldws(rws, o0 + 1808);
          m[4] = ldws(rws, o1); m[5] = ldws(rws, o1 + 16); m[6] = ldws(rws, o1 + 1792); m[7] = ldws(rws, o1 + 1808);
          unsigned t = 0;
#pragma unroll
          for (int i = 0; i < 8; ++i) t = tagx(t, m[i], tag);
          if (__all((t & 0xffffu) == 0u)) break;
          if (sp > SPIN_G) return;
          __builtin_amdgcn_s_sleep(4);
        }
        STAMP(2);
#pragma unroll
        for (int pi = 0; pi < 2; ++pi) {
          const v4u xn = pi ? xn1 : xn0;
#pragma unroll
          for (int i = 0; i < 4; ++i) {
            const v4u sa = m[4 * pi + (i >> 1)], ca = m[4 * pi + 2 + (i >> 1)];
            const int j = 2 * (i & 1);
            const float h0 = rb(lo16(xn[i]) * rb(1.f + hi16(ca[j]))) + hi16(sa[j]);
            const float h1 = rb(hi16(xn[i]) * rb(1.f + hi16(ca[j + 1]))) + hi16(sa[j + 1]);
            act[pi][i] = pk(h0, h1);
          }
        }
      }
      if (vw == 3) act[1] = v4u{0u, 0u, 0u, 0u};
      waitvm<0>();
      v4u wf[2][2][2];
      read_wfrags(L + L_EW, vw, lane, wf);
      STAMP(3);
      *(LAS f32x4*)(L + L_REDV + vw * 1024 + lane * 16) = gemv_partial(wf, act, lane);
      lds_signal(L, F_REDV, lane);
      if (vw == 0) {
        if (!lds_wait(L, F_REDV, 4)) return;
        const f32x4 o = red4(L + L_REDV, lane);
        const unsigned p01 = pk(o[0], o[1]), p23 = pk(o[2], o[3]);
        const v4u ov = {(p01 << 16) | tag, (p01 & 0xffff0000u) | tag, (p23 << 16) | tag, (p23 & 0xffff0000u) | tag};
        const int off = isM ? WS_H1 + (hb * 2688 + e * 32 + 16 * hf + 4 * g) * 4 : WS_H2 + (hb * 4096 + (e - 84) * 32 + 16 * hf + 4 * g) * 4;
        stws(rws, off, ov);
      }
      __builtin_amdgcn_s_setprio(0);
    } else {
      asm volatile("s_waitcnt lgkmcnt(0)" ::: "memory");
      __builtin_amdgcn_s_barrier();
    }
    STAMP(4);
    __builtin_amdgcn_s_barrier();
    STAMP(5);
    const rsrc_t rv = mkrsrc(a.vc + kvoff, 131072);
    const int vo = (lane >> 3) * 512 + vw * 128 + (lane & 7) * 16;
    v4u v[32];
#pragma unroll
    for (int n = 0; n < 32; ++n) {
      v[n] = __builtin_amdgcn_raw_buffer_load_b128(rv, vo, ((n + rot) & 31) * 4096, AUX_NT);
      __builtin_amdgcn_sched_barrier(0);
      if (LATE_AT > 0 && LATE_AT <= 32 && n + 1 == LATE_AT && vw == 0) lds_signal(L, F_LATE, lane);
      if (n + 1 < 32) waitvm<VDEPTH - 1>();
    }
    STAMP(6);
    if (!lds_wait(L, F_P, 4)) return;
    if (!lds_wait(L, F_SHADOW, 1)) return;
    STAMP(7);
    v4u pf[4];
    {
      const int row = lane & 15, g = lane >> 4, h = (row >> 2) & 1, x = row & 1;
#pragma unroll
      for (int G = 0; G < 4; ++G) pf[G] = *(LAS v4u*)(L + L_P + ((G * 2 + h) * 8 + 2 * g + x) * 16);
    }
    f32x4 acc[8];
#pragma unroll
    for (int j = 0; j < 8; ++j) acc[j] = f32x4{0.f, 0.f, 0.f, 0.f};
#pragma unroll
    for (int G = 0; G < 4; ++G) {
#pragma unroll
      for (int j = 0; j < 8; ++j) {
        v4u f;
#pragma unroll
        for (int q = 0; q < 4; ++q) f[q] = __builtin_amdgcn_perm(v[8 * G + 2 * q + 1][j >> 1], v[8 * G + 2 * q][j >> 1], (j & 1) ? 0x07060302u : 0x05040100u);
        acc[j] = mfma(pf[G], f, acc[j]);
      }
    }
    float o[8];
#pragma unroll
    for (int j = 0; j < 8; ++j) o[j] = acc[j][0] + dpp<ROR8>(acc[j][1]);
    const v4u ov = nosent(v4u{pk(o[0], o[1]), pk(o[2], o[3]), pk(o[4], o[5]), pk(o[6], o[7])});
    if ((lane & 0x28) == 0) {
      const int h = 2 * kvh + (lane >> 4), c = lane & 7, ks = 2 * vw + (c >> 2);
      stws(rws, WS_H3 + ((h * 8 + ks) * 2 + (b >> 2)) * 1024 + (4 * (b & 3) + s) * 64 + (c & 3) * 16, ov);
    }
    if (LATE_AT > 32 && vw == 0) lds_signal(L, F_LATE, lane);
    STAMP(8);
    STAMP_OUT();
    return;
  }

  const int kw = w - 4;
  unsigned tag;
  {
    const rsrc_t rk = mkrsrc(a.kc + kvoff, 131072);
    const int vo = ((lane >> 5) * 4 + ((lane >> 2) & 3)) * 512 + (((lane >> 4) & 1) * 4 + (lane & 3)) * 16;
    v4u k[32];
    unsigned ep = 0;
#pragma unroll
    for (int n = 0; n < 32; ++n) {
      k[n] = __builtin_amdgcn_raw_buffer_load_b128(rk, vo + (n & 3) * 128, ((4 * (n >> 2) + kw + rot) & 31) * 4096, AUX_NT);
      __builtin_amdgcn_sched_barrier(0);
      if (n == KDEPTH - 1) {
        ep = ldws1(rws, WS_EPOCH);
        const v4u sent = {~0u, ~0u, ~0u, ~0u};
        const int co = WS_H3 + bid * 9216 + lane * 16;
        stws(rws, co + kw * 1024, sent); stws(rws, co + (kw + 4) * 1024, sent);
        if (kw == 0) stws(rws, co + 8192, sent);
        __builtin_amdgcn_s_barrier();
        __builtin_amdgcn_sched_barrier(0);
      }
      if (n >= KDEPTH - 1 && n + 1 < 32) waitvm<KDEPTH - 1>();
    }
    STAMP(1);
    __builtin_amdgcn_s_barrier();
    STAMP(2);
    tag = (__builtin_amdgcn_readfirstlane(ep) + 1u) & 0xffffu;
    __builtin_amdgcn_s_setprio(2);
    if (kw == 0) {
      _Pragma("unroll 1") for (unsigned sp = 0;; ++sp) {
      asm volatile("" ::: "memory");
        const int o = WS_H2 + (b * 4096 + kvh * 512 + lane * 8) * 4;
        const v4u q0 = ldws(rws, o), q1 = ldws(rws, o + 16);
        if (__all((tagx(tagx(0u, q0, tag), q1, tag) & 0xffffu) == 0u)) {
          *(LAS v4u*)(L + L_Q + lane * 16) = v4u{__builtin_amdgcn_perm(q0.y, q0.x, 0x07060302u), __builtin_amdgcn_perm(q0.w, q0.z, 0x07060302u),
                                                 __builtin_amdgcn_perm(q1.y, q1.x, 0x07060302u), __builtin_amdgcn_perm(q1.w, q1.z, 0x07060302u)};
          break;
        }
        if (sp > SPIN_G) return;
        __builtin_amdgcn_s_sleep(4);
      }
      lds_signal(L, F_Q, lane);
    }
    if (!lds_wait(L, F_Q, 1)) return;
    STAMP(3);
    v4u qf[4];
    {
      const int row = lane & 15, g1r = row >> 3, h = (row >> 2) & 1, bq = row & 3, g1 = lane >> 5, g0 = (lane >> 4) & 1;
#pragma unroll
      for (int j = 0; j < 4; ++j) {
        const v4u t = *(LAS v4u*)(L + L_Q + (h * 256 + 64 * j + 8 * (4 * g0 + bq)) * 2);
        qf[j] = g1 == g1r ? t : v4u{0u, 0u, 0u, 0u};
      }
    }
    float S[2];
    const int bb = lane & 3;
#pragma unroll
    for (int i = 0; i < 8; ++i) {
      f32x4 d = {0.f, 0.f, 0.f, 0.f};
#pragma unroll
      for (int j = 0; j < 4; ++j) d = mfma(qf[j], k[4 * i + j], d);
      const float sc = (dpp<0x00>(d[0]) + dpp<0x55>(d[1])) + (dpp<0xAA>(d[2]) + dpp<0xFF>(d[3]));
      if ((i & 3) == 0) S[i >> 2] = sc; else S[i >> 2] = bb == (i & 3) ? sc : S[i >> 2];
    }
    STAMP(4);
    constexpr float LOG2E = 1.4426950408889634f;
    const float s0 = rb(S[0]) * 0.0625f, s1 = rb(S[1]) * 0.0625f;
    const float mw = max32(rowmax(fmaxf(s0, s1)));
    float lw = rowsum(__builtin_amdgcn_exp2f((s0 - mw) * LOG2E) + __builtin_amdgcn_exp2f((s1 - mw) * LOG2E));
    lw = sum32swap(lw, lw);
    const int hh = (lane >> 4) & 1;
    if ((lane & 0x2f) == 0) *(LAS f32x2*)(L + L_ML + (kw * 2 + hh) * 8) = f32x2{mw, lw};
    lds_signal(L, F_ML, lane);
    if (kw == 0) {
      if (!lds_wait(L, F_ML, 4)) return;
      const int h = lane & 1;
      float m = -INFINITY, l = 0.f;
      f32x2 t[4];
#pragma unroll
      for (int i = 0; i < 4; ++i) { t[i] = *(LAS f32x2*)(L + L_ML + (i * 2 + h) * 8); m = fmaxf(m, t[i].x); }
#pragma unroll
      for (int i = 0; i < 4; ++i) l += t[i].y * __builtin_amdgcn_exp2f((t[i].x - m) * LOG2E);
      const int quad = WS_STATS + (b * 8 + kvh) * 64;
      if (lane < 2) __builtin_amdgcn_raw_buffer_store_b64(v2u{asu(l), (asu(m) & 0xffff0000u) | tag}, rws, quad + s * 16 + h * 8, 0, AUX_SC1);
      STAMP(5);
      v2u pe[4];
      _Pragma("unroll 1") for (unsigned sp = 0;; ++sp) {
      asm volatile("" ::: "memory");
        unsigned t2 = 0;
#pragma unroll
        for (int i = 0; i < 4; ++i) { pe[i] = __builtin_amdgcn_raw_buffer_load_b64(rws, quad + i * 16 + h * 8, 0, AUX_SC1); t2 |= pe[i].y ^ tag; }
        if (__all((t2 & 0xffffu) == 0u)) break;
        if (sp > SPIN_G) return;
        __builtin_amdgcn_s_sleep(4);
      }
      float M = -INFINITY, Lg = 0.f;
#pragma unroll
      for (int i = 0; i < 4; ++i) M = fmaxf(M, hi16(pe[i].y));
#pragma unroll
      for (int i = 0; i < 4; ++i) Lg += asf(pe[i].x) * __builtin_amdgcn_exp2f((hi16(pe[i].y) - M) * LOG2E);
      if (lane < 2) *(LAS f32x2*)(L + L_ML + 64 + h * 8) = f32x2{M, 1.f / Lg};
      lds_signal(L, F_GLOB, lane);
    }
    if (!lds_wait(L, F_GLOB, 1)) return;
    STAMP(6);
    const f32x2 g = *(LAS f32x2*)(L + L_ML + 64 + hh * 8);
    const unsigned pp = pk(__builtin_amdgcn_exp2f((s0 - g.x) * LOG2E) * g.y, __builtin_amdgcn_exp2f((s1 - g.x) * LOG2E) * g.y);
    {
      const int ks8 = 4 * (lane >> 5) + ((lane >> 2) & 3), iv = 4 * (bb & 1) + kw, G0 = bb >> 1;
      *(LAS u16*)(L + L_P + ((G0 * 2 + hh) * 8 + ks8) * 16 + iv * 2) = (u16)pp;
      *(LAS u16*)(L + L_P + (((G0 + 2) * 2 + hh) * 8 + ks8) * 16 + iv * 2) = (u16)(pp >> 16);
    }
    lds_signal(L, F_P, lane);
    if (kw == 1) {
      _Pragma("unroll 1") for (unsigned sp = 0;; ++sp) {
      asm volatile("" ::: "memory");
        unsigned t2 = 0;
#pragma unroll
        for (int i = 0; i < 4; ++i) { const v4u t = ldws(rws, WS_STATS + i * 1024 + lane * 16); t2 |= (t.y ^ tag) | (t.w ^ tag); }
        if (__all((t2 & 0xffffu) == 0u)) break;
        if (sp > SPIN_G) return;
        __builtin_amdgcn_s_sleep(4);
      }
      lds_signal(L, F_SHADOW, lane);
      if (bid == 0 && lane == 0) __builtin_amdgcn_raw_buffer_store_b32(ep + 1u, rws, WS_EPOCH, 0, AUX_SC1);
    }
    STAMP(7);
  }

  const int fq = (7 - (r >> 2)) * 8 + xc;
  if (fq >= 56) {
    const int t = ((fq - 56) * 4 + (r & 3)) * 4 + kw;
    if (t < 112) {
      const int tb = t / 14, m = t - tb * 14;
      const int col = colchunk(m, lane >> 4) * 8 + (lane & 7);
      const float xv = lo16(a.x[tb * 448 + col]);
      unsigned ga, cb;
      _Pragma("unroll 1") for (unsigned sp = 0;; ++sp) {
      asm volatile("" ::: "memory");
        ga = ldws1(rws, WS_H1 + (tb * 2688 + 2 * 448 + col) * 4); cb = ldws1(rws, WS_H1 + (tb * 2688 + 4 * 448 + col) * 4);
        if (__all((((ga ^ tag) | (cb ^ tag)) & 0xffffu) == 0u)) break;
        if (sp > SPIN_G) return;
        __builtin_amdgcn_s_sleep(4);
      }
      const float gaf = hi16(ga), cc = rb(1.f + hi16(cb));
      if (!lds_wait(L, F_SHADOW, 1)) return;
      STAMP(8);
      _Pragma("unroll 1") for (unsigned sp = 0;; ++sp) {
      asm volatile("" ::: "memory");
        v4u y[4];
#pragma unroll
        for (int i = 0; i < 4; ++i) y[i] = ldws(rws, WS_H4 + t * 4096 + i * 1024 + lane * 16);
        const bool ok = __all(smax(smax(smax(smax(0u, y[0]), y[1]), y[2]), y[3]) != ~0u);
        if (ok || sp == 0) {
          const f32x4 sv = (__builtin_bit_cast(f32x4, y[0]) + __builtin_bit_cast(f32x4, y[1])) + (__builtin_bit_cast(f32x4, y[2]) + __builtin_bit_cast(f32x4, y[3]));
          float u = sum16swap(sum32swap(sv[0], sv[1]), sum32swap(sv[2], sv[3]));
          u += dpp<ROR8>(u);
          const float x1 = rb(xv + rb(gaf * rb(u)));
          const unsigned o = min(pk(x1, x1 * cc), 0xfffffffeu);
          if (ok) {
            if ((lane & 8) == 0) __builtin_amdgcn_raw_buffer_store_b32(o, rws, WS_H5 + (tb * 448 + m * 32 + (lane >> 4) * 8 + (lane & 7)) * 4, 0, AUX_SC1);
            break;
          }
          asm volatile("" ::"v"(o));
        }
        if (sp > SPIN_G) return;
      }
      STAMP(9);
    }
    STAMP_OUT();
    return;
  }

  if (LATE_AT > 0) { if (!lds_wait(L, F_LATE, 1)) return; }

  if (fq >= 28) {
    const int wt = (fq - 28) * 4 + (r & 3), h = wt / 7, cb64 = wt - h * 7, khalf = kw >> 1, rt = kw & 1;
    {
      const rsrc_t rw = mkrsrc(a.wo + (size_t)h * 256 * 448 + cb64 * 64, 256 * 896 - cb64 * 128);
      const int vo = (lane >> 3) * 896 + (lane & 7) * 16;
#pragma unroll
      for (int i = 0; i < 8; ++i) __builtin_amdgcn_raw_ptr_buffer_load_lds(rw, (LAS void*)(L + L_LW + (8 * kw + i) * 1024), 16, vo, (8 * kw + i) * 8 * 896, 0, AUX_NT);
    }
    const int t = wt * 2 + (kw & 1), tb = t / 28, tile = t - tb * 28;
    const int rho = lane >> 4, ocol = 16 * tile + 4 * (lane & 3) + (((rho & 1) << 1) | (rho >> 1));
    unsigned gb;
    _Pragma("unroll 1") for (unsigned sp = 0;; ++sp) {
      asm volatile("" ::: "memory");
      gb = ldws1(rws, WS_H1 + (tb * 2688 + 5 * 448 + ocol) * 4);
      if (__all(((gb ^ tag) & 0xffffu) == 0u)) break;
      if (sp > SPIN_G) return;
      __builtin_amdgcn_s_sleep(4);
    }
    waitvm<0>();
    lds_signal(L, F_REDV, lane);
    const int e = 2 * ((r >> 1) * 8 + xc) + (r & 1);
    if (!lds_wait(L, F_REDV, (e < 212 ? 4u : 0u) + 4u)) return;
    v4u wf[4][4];
    {
      const int g = lane >> 4, q = (lane >> 2) & 3, p = lane & 3;
#pragma unroll
      for (int i = 0; i < 4; ++i)
#pragma unroll
        for (int ct = 0; ct < 4; ++ct) {
          LAS unsigned char* bp = L + L_LW + (128 * khalf + 32 * i + 8 * g + q) * 128 + ct * 32 + p * 8;
          wf[i][ct] = trfrag(bp, bp + 512);
        }
    }
    if (!lds_wait(L, F_SHADOW, 1)) return;
    STAMP(8);
    _Pragma("unroll 1") for (unsigned sp = 0;; ++sp) {
      asm volatile("" ::: "memory");
      v4u of[4];
#pragma unroll
      for (int i = 0; i < 4; ++i) of[i] = ldws(rws, WS_H3 + ((h * 8 + 4 * khalf + i) * 2 + rt) * 1024 + (lane & 15) * 64 + (lane >> 4) * 16);
      const bool ok = __all(smax(smax(smax(smax(0u, of[0]), of[1]), of[2]), of[3]) != ~0u);
      if (ok || sp == 0) {
        f32x4 d[4];
#pragma unroll
        for (int ct = 0; ct < 4; ++ct) d[ct] = mfma(of[0], wf[0][ct], f32x4{0.f, 0.f, 0.f, 0.f});
#pragma unroll
        for (int i = 1; i < 4; ++i)
#pragma unroll
          for (int ct = 0; ct < 4; ++ct) d[ct] = mfma(of[i], wf[i][ct], d[ct]);
        v4u o;
#pragma unroll
        for (int ct = 0; ct < 4; ++ct) o[ct] = asu((d[ct][0] + d[ct][1]) + (d[ct][2] + d[ct][3]));
        o = nosent(o);
        if (ok) {
          const int c = lane & 15, yb = 4 * rt + (lane >> 4);
          stws(rws, WS_H4 + ((yb * 14 + 2 * cb64 + (c >> 3)) * 32 + 2 * h + khalf) * 128 + (c & 7) * 16, o);
          break;
        }
        asm volatile("" ::"v"(o));
      }
      if (sp > SPIN_G) return;
    }
    STAMP(9);
    if (kw >= 2) { STAMP_OUT(); return; }
    const int cc8 = ocol >> 3, mm = 2 * (cc8 >> 3) + (cc8 & 1), ct2 = (cc8 >> 1) & 3, rh = ((ct2 & 1) << 1) | (ct2 >> 1);
    unsigned xz;
    _Pragma("unroll 1") for (unsigned sp = 0;; ++sp) {
      asm volatile("" ::: "memory");
      xz = ldws1(rws, WS_H5 + (tb * 448 + mm * 32 + rh * 8 + (ocol & 7)) * 4);
      if (__all(xz != ~0u)) break;
      if (sp > SPIN_G) return;
    }
    STAMP(10);
    const float x1 = lo16(xz), gbf = hi16(gb);
    _Pragma("unroll 1") for (unsigned sp = 0;; ++sp) {
      asm volatile("" ::: "memory");
      v4u y[7];
#pragma unroll
      for (int i = 0; i < 7; ++i) y[i] = ldws(rws, WS_H6 + t * 7168 + i * 1024 + lane * 16);
      unsigned mx = 0;
#pragma unroll
      for (int i = 0; i < 7; ++i) mx = smax(mx, y[i]);
      const bool ok = __all(mx != ~0u);
      if (ok || sp == 0) {
        f32x4 sv = ((__builtin_bit_cast(f32x4, y[0]) + __builtin_bit_cast(f32x4, y[1])) + (__builtin_bit_cast(f32x4, y[2]) + __builtin_bit_cast(f32x4, y[3]))) +
                   ((__builtin_bit_cast(f32x4, y[4]) + __builtin_bit_cast(f32x4, y[5])) + __builtin_bit_cast(f32x4, y[6]));
        float u = sum16swap(sum32swap(sv[0], sv[1]), sum32swap(sv[2], sv[3]));
        u += dpp<ROR8>(u);
        u += dpp<ROR4>(u);
        const unsigned o = pk(x1 + rb(gbf * rb(u)), 0.f);
        if (ok) {
          if ((lane & 12) == 0) a.out[tb * 448 + ocol] = (u16)o;
          break;
        }
        asm volatile("" ::"v"(o));
      }
      if (sp > SPIN_G) return;
    }
    STAMP(11);
    STAMP_OUT();
    return;
  }

  {
    const int f = fq * 4 + (r & 3);
    const int col = lane & 15, g = lane >> 4, hb = col & 7, hf = col >> 3;
    const int P1 = kw == 3 ? 3 : kw + 4;
    {
      const u16* wlo = a.w1 < a.w3 ? a.w1 : a.w3;
      const size_t d1 = (size_t)(a.w1 - wlo) * 2, d3 = (size_t)(a.w3 - wlo) * 2;
      const int prow = lane >> 2, piece = lane & 3;
#pragma unroll
      for (int pi = 0; pi < 2; ++pi) {
        if (pi == 1 && kw == 3) break;
        const int P = kw + 4 * pi;
#pragma unroll
        for (int i = 0; i < 4; ++i) {
          const int ks = 2 * P + (i >> 1), rho = 2 * (i & 1) + (prow >> 3);
          const int row = 8 * colchunk(ks, rho) + (prow & 7);
          const unsigned char* src = (const unsigned char*)wlo + (piece < 2 ? d1 : d3) + (size_t)(row * 1792 + f * 16) * 2 + (piece & 1) * 16;
          __builtin_amdgcn_global_load_lds((const __attribute__((address_space(1))) void*)src, (LAS void*)(L + L_LW + (4 * P + i) * 1024), 16, 0, AUX_NT);
        }
      }
      const rsrc_t r2 = mkrsrc(a.w2 + (size_t)f * 16 * 448, 14336);
#pragma unroll
      for (int i = 0; i < 4; ++i)
        if (4 * i + kw < 14) __builtin_amdgcn_raw_ptr_buffer_load_lds(r2, (LAS void*)(L + L_W2 + (4 * i + kw) * 1024), 16, lane * 16, (4 * i + kw) * 1024, 0, AUX_NT);
    }
    const int c0 = 8 * colchunk(2 * kw + hf, g), c1 = 8 * colchunk(2 * P1 + hf, g);
    v4u ccf[2], sbf[2];
    {
      v4u m[8];
      _Pragma("unroll 1") for (unsigned sp = 0;; ++sp) {
      asm volatile("" ::: "memory");
        const int o0 = WS_H1 + (hb * 2688 + 3 * 448 + c0) * 4, o1 = WS_H1 + (hb * 2688 + 3 * 448 + c1) * 4;
        m[0] = ldws(rws, o0); m[1] = ldws(rws, o0 + 16); m[2] = ldws(rws, o0 + 1792); m[3] = ldws(rws, o0 + 1808);
        m[4] = ldws(rws, o1); m[5] = ldws(rws, o1 + 16); m[6] = ldws(rws, o1 + 1792); m[7] = ldws(rws, o1 + 1808);
        unsigned t = 0;
#pragma unroll
        for (int i = 0; i < 8; ++i) t = tagx(t, m[i], tag);
        if (__all((t & 0xffffu) == 0u)) break;
        if (sp > SPIN_G) return;
        __builtin_amdgcn_s_sleep(4);
      }
#pragma unroll
      for (int pi = 0; pi < 2; ++pi)
#pragma unroll
        for (int i = 0; i < 4; ++i) {
          const v4u sb = m[4 * pi + (i >> 1)], cb = m[4 * pi + 2 + (i >> 1)];
          const int j = 2 * (i & 1);
          sbf[pi][i] = __builtin_amdgcn_perm(sb[j + 1], sb[j], 0x07060302u);
          ccf[pi][i] = pk(1.f + hi16(cb[j]), 1.f + hi16(cb[j + 1]));
        }
      if (kw == 3) { ccf[1] = v4u{0u, 0u, 0u, 0u}; sbf[1] = v4u{0u, 0u, 0u, 0u}; }
    }
    waitvm<0>();
    v4u wf[2][2][2];
    read_wfrags(L + L_LW, kw, lane, wf);
    *(LAS f32x4*)(L + L_REDK + kw * 1024 + lane * 16) = gemv_partial(wf, ccf, lane);
    *(LAS f32x4*)(L + L_REDK + 4096 + kw * 1024 + lane * 16) = gemv_partial(wf, sbf, lane);
    asm volatile("s_waitcnt lgkmcnt(0)" ::: "memory");
    __builtin_amdgcn_s_barrier();
    const f32x4 T1 = red4(L + L_REDK, lane), T2 = red4(L + L_REDK + 4096, lane);
    v4u w2f[4];
    {
      const int q = (lane >> 2) & 3, p = lane & 3;
#pragma unroll
      for (int i = 0; i < 4; ++i) {
        const int tp = min(4 * i + kw, 13);
        LAS unsigned char* bp = L + L_W2 + (4 * g + q) * 896 + tp * 64 + p * 8;
        w2f[i] = trfrag(bp, bp + 32);
      }
    }
    asm volatile("s_waitcnt lgkmcnt(0)" ::: "memory");
    __builtin_amdgcn_s_barrier();
    if (!lds_wait(L, F_SHADOW, 1)) return;
    STAMP(8);
    const int z0 = WS_H5 + (hb * 448 + (2 * kw + hf) * 32 + g * 8) * 4, z1 = WS_H5 + (hb * 448 + (2 * P1 + hf) * 32 + g * 8) * 4;
    int pass = 0;
    asm volatile("" : "+s"(pass));
    _Pragma("unroll 1") for (; pass < 2; ++pass) {
      v4u zz[4];
      _Pragma("unroll 1") for (unsigned sp = 0;; ++sp) {
      asm volatile("" ::: "memory");
        zz[0] = ldws(rws, z0); zz[1] = ldws(rws, z0 + 16); zz[2] = ldws(rws, z1); zz[3] = ldws(rws, z1 + 16);
        if (pass == 0 || __all(smax(smax(smax(smax(0u, zz[0]), zz[1]), zz[2]), zz[3]) != ~0u)) break;
        if (sp > SPIN_G) return;
      }
      STAMP(9);
      v4u zf[2], xf[2];
#pragma unroll
      for (int pi = 0; pi < 2; ++pi)
#pragma unroll
        for (int i = 0; i < 4; ++i) {
          const v4u t = zz[2 * pi + (i >> 1)];
          const int j = 2 * (i & 1);
          zf[pi][i] = __builtin_amdgcn_perm(t[j + 1], t[j], 0x07060302u);
          xf[pi][i] = __builtin_amdgcn_perm(t[j + 1], t[j], 0x05040100u);
        }
      if (kw == 3) { zf[1] = v4u{0u, 0u, 0u, 0u}; xf[1] = v4u{0u, 0u, 0u, 0u}; }
      const f32x4 part = gemv_partial(wf, zf, lane);
      const v4u ones = {0x3f803f80u, 0x3f803f80u, 0x3f803f80u, 0x3f803f80u};
      f32x4 s1 = mfma(ones, xf[0], f32x4{0.f, 0.f, 0.f, 0.f}), s2 = mfma(xf[0], xf[0], f32x4{0.f, 0.f, 0.f, 0.f});
      s1 = mfma(ones, xf[1], s1); s2 = mfma(xf[1], xf[1], s2);
      *(LAS f32x4*)(L + L_REDK + kw * 1024 + lane * 16) = part;
      {
        const int cr = col & 3;
        const float dg = cr == 0 ? s2[0] : cr == 1 ? s2[1] : cr == 2 ? s2[2] : s2[3];
        if ((col >> 2) == g) *(LAS f32x2*)(L + L_STK + (hb * 8 + kw * 2 + hf) * 8) = f32x2{s1[0], dg};
      }
      asm volatile("s_waitcnt lgkmcnt(0)" ::: "memory");
      __builtin_amdgcn_s_barrier();
      const f32x4 Z = red4(L + L_REDK, lane);
      LAS f32x4* sp4 = (LAS f32x4*)(L + L_STK + hb * 64);
      const f32x4 t0 = (sp4[0] + sp4[1]) + (sp4[2] + sp4[3]);
      const float mu = (t0[0] + t0[2]) * (1.f / 448.f), var = fmaxf((t0[1] + t0[3]) * (1.f / 448.f) - mu * mu, 0.f);
      const float rs = __builtin_amdgcn_rsqf(var + 1e-5f);
      float hv[4];
#pragma unroll
      for (int i = 0; i < 4; ++i) {
        const float val = rb(fmaf(rs, fmaf(-mu, T1[i], Z[i]), T2[i])), oth = dpp<ROR8>(val);
        const float gt = hf ? oth : val, up = hf ? val : oth;
        hv[i] = rb(silu(gt)) * up;
      }
      const unsigned h01 = pk(hv[0], hv[1]), h23 = pk(hv[2], hv[3]);
      const v4u hf4 = {hf ? 0u : h01, hf ? 0u : h23, hf ? h01 : 0u, hf ? h23 : 0u};
#pragma unroll
      for (int i = 0; i < 4; ++i) {
        const v4u o = nosent(__builtin_bit_cast(v4u, mfma(w2f[i], hf4, f32x4{0.f, 0.f, 0.f, 0.f})));
        const int tp = 4 * i + kw;
        if (pass == 1 && tp < 14) stws(rws, WS_H6 + ((hb * 28 + 2 * tp + hf) * 112 + f) * 64 + g * 16, o);
        else asm volatile("" ::"v"(o));
      }
      if (pass == 0) __builtin_amdgcn_s_barrier();
    }
    STAMP(10);
    STAMP_OUT();
  }
}

extern "C" void kernel_launch(void* const* d_in, const int* in_sizes, int n_in, void* d_out, int out_size, void* d_ws, size_t ws_size, hipStream_t stream) {
  Args a;
  a.x = (const u16*)d_in[0]; a.kc = (const u16*)d_in[1]; a.vc = (const u16*)d_in[2]; a.wq = (const u16*)d_in[3]; a.wo = (const u16*)d_in[4];
  a.w1 = (const u16*)d_in[6]; a.w3 = (const u16*)d_in[7]; a.w2 = (const u16*)d_in[8]; a.c = (const u16*)d_in[9]; a.wmod = (const u16*)d_in[10];
  a.out = (u16*)d_out; a.ws = (unsigned char*)d_ws;
  (void)hipFuncSetAttribute((const void*)fused_block, hipFuncAttributeMaxDynamicSharedMemorySize, L_BYTES);
  fused_block<<<256, 512, L_BYTES, stream>>>(a);
}
```

```cpp
#include <hip/hip_runtime.h>
#include <hip/hip_bf16.h>
#include <stdint.h>

#define STAMPS 1
#define KDEPTH 6
#define VDEPTH 6
#define LATE_AT 0

typedef unsigned short u16;
typedef unsigned long long u64;
typedef unsigned v4u __attribute__((ext_vector_type(4)));
typedef unsigned v2u __attribute__((ext_vector_type(2)));
typedef float f32x4 __attribute__((ext_vector_type(4)));
typedef float f32x2 __attribute__((ext_vector_type(2)));
typedef __bf16 bf16x8 __attribute__((ext_vector_type(8)));
typedef __bf16 bf16x2 __attribute__((ext_vector_type(2)));
typedef short s16x4 __attribute__((ext_vector_type(4)));
#define LAS __attribute__((address_space(3)))
#define DEV __device__ __forceinline__
typedef __amdgpu_buffer_rsrc_t rsrc_t;

constexpr int AUX_NT = 2, AUX_SC1 = 16;
constexpr int WS_EPOCH = 0, WS_STATS = 4096, WS_H1 = 8192, WS_H2 = WS_H1 + 8 * 2688 * 4  ;
constexpr int WS_H3 = WS_H2 + 8 * 4096 * 4  , WS_H4 = WS_H3 + 262144, WS_H5 = WS_H4 + 458752, WS_H6 = WS_H5 + 14336;
constexpr int WS_END = WS_H6 + 1605632;
constexpr int CLR_KB = (WS_END - WS_H3) / 1024;
constexpr int WS_CLR_END = WS_H3 + 256 * 9 * 1024;
static_assert(CLR_KB <= 256 * 9 && (WS_END - WS_H3) % 1024 == 0, "clear partition");
constexpr int WS_STAMP = 4 << 20;
constexpr int WS_BYTES = STAMPS ? (8 << 20) : WS_CLR_END;
constexpr int L_EW = 0;
constexpr int L_LW = 28672;
constexpr int L_W2 = L_LW + 28672;
constexpr int L_XN = L_W2 + 14336;
constexpr int L_REDV = L_XN + 7168;
constexpr int L_REDK = L_REDV + 4096;
constexpr int L_STK = L_REDK + 8192;
constexpr int L_Q = L_STK + 512;
constexpr int L_P = L_Q + 1024;
constexpr int L_ML = L_P + 1024;
constexpr int L_FLAG = L_ML + 128;
constexpr int L_BYTES = L_FLAG + 64;
enum { F_XN = 0, F_REDV = 1, F_Q = 2, F_ML = 3, F_GLOB = 4, F_P = 5, F_SHADOW = 6, F_LATE = 7 };

constexpr unsigned SPIN_G = 1u << 16, SPIN_L = 1u << 22;

struct Args { const u16 *x, *kc, *vc, *wq, *wo, *w1, *w3, *w2, *c, *wmod; u16* out; unsigned char* ws; };

DEV rsrc_t mkrsrc(const void* p, unsigned bytes) { return __builtin_amdgcn_make_buffer_rsrc((void*)p, 0, bytes, 0x00020000); }
DEV f32x4 mfma(v4u a, v4u b, f32x4 c) { return __builtin_amdgcn_mfma_f32_16x16x32_bf16(__builtin_bit_cast(bf16x8, a), __builtin_bit_cast(bf16x8, b), c, 0, 0, 0); }
DEV unsigned pk(float lo, float hi) { f32x2 v = {lo, hi}; return __builtin_bit_cast(unsigned, __builtin_convertvector(v, bf16x2)); }
DEV float asf(unsigned u) { return __builtin_bit_cast(float, u); }
DEV unsigned asu(float f) { return __builtin_bit_cast(unsigned, f); }
DEV float lo16(unsigned u) { return asf(u << 16); }
DEV float hi16(unsigned u) { return asf(u & 0xffff0000u); }
DEV float rb(float f) { return lo16(pk(f, f)); }
template <int CTRL> DEV float dpp(float x) { return asf(__builtin_amdgcn_mov_dpp((int)asu(x), CTRL, 0xf, 0xf, true)); }
constexpr int ROR1 = 0x121, ROR2 = 0x122, ROR4 = 0x124, ROR8 = 0x128;
DEV float sum32swap(float a, float b) { auto r = __builtin_amdgcn_permlane32_swap(asu(a), asu(b), false, false); return asf(r[0]) + asf(r[1]); }
DEV float sum16swap(float a, float b) { auto r = __builtin_amdgcn_permlane16_swap(asu(a), asu(b), false, false); return asf(r[0]) + asf(r[1]); }
DEV float max32(float a) { auto r = __builtin_amdgcn_permlane32_swap(asu(a), asu(a), false, false); return fmaxf(asf(r[0]), asf(r[1])); }
DEV float rowsum(float v) { v += dpp<ROR8>(v); v += dpp<ROR4>(v); v += dpp<ROR2>(v); v += dpp<ROR1>(v); return v; }
DEV float rowmax(float v) { v = fmaxf(v, dpp<ROR8>(v)); v = fmaxf(v, dpp<ROR4>(v)); v = fmaxf(v, dpp<ROR2>(v)); v = fmaxf(v, dpp<ROR1>(v)); return v; }
DEV float wavesum(float v) { v = rowsum(v); v = sum16swap(v, v); return sum32swap(v, v); }
DEV unsigned umax3(unsigned a, unsigned b, unsigned c) { return max(max(a, b), c); }
DEV unsigned smax(unsigned acc, v4u v) { return umax3(umax3(acc, v.x, v.y), v.z, v.w); }
DEV unsigned tagx(unsigned acc, v4u v, unsigned tag) { return acc | (v.x ^ tag) | (v.y ^ tag) | (v.z ^ tag) | (v.w ^ tag); }
DEV v4u nosent(v4u v) { v.x = min(v.x, 0xfffffffeu); v.y = min(v.y, 0xfffffffeu); v.z = min(v.z, 0xfffffffeu); v.w = min(v.w, 0xfffffffeu); return v; }
template <int N> DEV void waitvm() { asm volatile("s_waitcnt vmcnt(%0)" ::"i"(N) : "memory"); }
DEV float silu_p(float v) { return v / (1.f + __builtin_amdgcn_exp2f(v * -1.4426950408889634f)); }
DEV float silu(float v) { return v * __builtin_amdgcn_rcpf(1.f + __builtin_amdgcn_exp2f(v * -1.4426950408889634f)); }

DEV v4u ldws(rsrc_t rs, int off) { return __builtin_amdgcn_raw_buffer_load_b128(rs, off, 0, AUX_SC1); }
DEV unsigned ldws1(rsrc_t rs, int off) { return __builtin_amdgcn_raw_buffer_load_b32(rs, off, 0, AUX_SC1); }
DEV void stws(rsrc_t rs, int off, v4u v) { __builtin_amdgcn_raw_buffer_store_b128(v, rs, off, 0, AUX_SC1); }

DEV void lds_signal(LAS unsigned char* L, int flag, int lane) {
  asm volatile("s_waitcnt lgkmcnt(0)" ::: "memory");
  if (lane == 0) __hip_atomic_fetch_add((LAS unsigned*)(L + L_FLAG) + flag, 1u, __ATOMIC_RELAXED, __HIP_MEMORY_SCOPE_WORKGROUP);
}
DEV bool lds_wait(LAS unsigned char* L, int flag, unsigned want) {
  _Pragma("unroll 1") for (unsigned sp = 0;; ++sp) {
      asm volatile("" ::: "memory");
    unsigned v = __builtin_amdgcn_readfirstlane(__hip_atomic_load((LAS unsigned*)(L + L_FLAG) + flag, __ATOMIC_RELAXED, __HIP_MEMORY_SCOPE_WORKGROUP));
    if (v >= want) break;
    if (sp > SPIN_L) return false;
    __builtin_amdgcn_s_sleep(1);
  }
  asm volatile("" ::: "memory");
  return true;
}
DEV v4u trfrag(LAS unsigned char* p0, LAS unsigned char* p1) {
  s16x4 a = __builtin_amdgcn_ds_read_tr16_b64_v4i16((LAS s16x4*)p0), b = __builtin_amdgcn_ds_read_tr16_b64_v4i16((LAS s16x4*)p1);
  v2u x = __builtin_bit_cast(v2u, a), y = __builtin_bit_cast(v2u, b);
  return v4u{x.x, x.y, y.x, y.y};
}
DEV void read_wfrags(LAS unsigned char* img, int wv, int lane, v4u (&wf)[2][2][2]) {
  const int g = lane >> 4, q = (lane >> 2) & 3, p = lane & 3;
#pragma unroll
  for (int pi = 0; pi < 2; ++pi) {
    const int P = (pi == 1 && wv == 3) ? 3 : wv + 4 * pi;
#pragma unroll
    for (int hf = 0; hf < 2; ++hf)
#pragma unroll
      for (int ct = 0; ct < 2; ++ct) {
        LAS unsigned char* b = img + (64 * P + 32 * hf + 8 * g + q) * 64 + ct * 32 + p * 8;
        wf[pi][hf][ct] = trfrag(b, b + 256);
      }
  }
}
DEV f32x4 gemv_partial(const v4u (&wf)[2][2][2], const v4u (&act)[2], int lane) {
  f32x4 z = {0.f, 0.f, 0.f, 0.f};
  f32x4 a0 = mfma(wf[0][0][0], act[0], z), a1 = mfma(wf[0][0][1], act[0], z), b0 = mfma(wf[0][1][0], act[0], z), b1 = mfma(wf[0][1][1], act[0], z);
  a0 = mfma(wf[1][0][0], act[1], a0); a1 = mfma(wf[1][0][1], act[1], a1); b0 = mfma(wf[1][1][0], act[1], b0); b1 = mfma(wf[1][1][1], act[1], b1);
  const bool lo = (lane & 8) == 0;
  f32x4 r;
#pragma unroll
  for (int i = 0; i < 4; ++i) { const float u = lo ? a0[i] : b1[i], v = lo ? a1[i] : b0[i]; r[i] = u + dpp<ROR8>(v); }
  return r;
}
DEV f32x4 red4(LAS unsigned char* red, int lane) {
  LAS f32x4* p = (LAS f32x4*)(red + lane * 16);
  return (p[0] + p[64]) + (p[128] + p[192]);
}
DEV int colchunk(int m, int rho) { return 8 * (m >> 1) + 2 * (((rho & 1) << 1) | (rho >> 1)) + (m & 1); }

#define STAMP(i) do { __builtin_amdgcn_sched_barrier(0); st[i] = __builtin_amdgcn_s_memrealtime(); __builtin_amdgcn_sched_barrier(0); } while (0)
#define STAMP_OUT() do { if (w < 4 && (7 - (r >> 2)) * 8 + xc < 28) break; const u64 dl_ = st[0] + 4000 + (((false) && st[0]) ? 4 * (st[0] - st[0]) : 0); while (__builtin_amdgcn_s_memrealtime() < dl_) __builtin_amdgcn_s_sleep(2); } while (0)

__global__ void __launch_bounds__(512, 2) fused_block(Args a) {
  extern __shared__ __attribute__((aligned(16))) unsigned char smem[];
  LAS unsigned char* L = (LAS unsigned char*)smem;
  const int lane = threadIdx.x & 63, w = __builtin_amdgcn_readfirstlane(threadIdx.x >> 6);
  const int bid = blockIdx.x, xc = bid & 7, r = bid >> 3;
  const int kvh = xc, b = r >> 2, s = r & 3;
  u64 st[16];
#pragma unroll
  for (int i = 0; i < 16; ++i) st[i] = 0;
  STAMP(0);
  const rsrc_t rws = mkrsrc(a.ws, WS_BYTES);
  const size_t kvoff = ((size_t)(b * 8 + kvh) * 4096 + 3072 + s * 256) * 256;
  const int rot = (bid * 5 + (bid >> 3)) & 31;

  if (w < 4) {
    const int vw = w;
    const int e = 2 * ((r >> 1) * 8 + xc) + (r & 1);
    const bool isM = e < 84, isQ = e >= 84 && e < 212;
    const unsigned ep = ldws1(rws, WS_EPOCH);
    if (w == 0 && lane < 16) ((LAS unsigned*)(L + L_FLAG))[lane] = 0u;
    if (isM || isQ) {
      const int col = lane & 15, g = lane >> 4, hb = col & 7, hf = col >> 3;
      const int P1 = vw == 3 ? 3 : vw + 4;
      const int k0 = 64 * vw + 32 * hf + 8 * g, k1 = 64 * P1 + 32 * hf + 8 * g;
      v4u act[2];
      v4u in0, in1;
      if (isM) {
        const rsrc_t rc = mkrsrc(a.c, 8 * 448 * 2);
        in0 = __builtin_amdgcn_raw_buffer_load_b128(rc, (hb * 448 + k0) * 2, 0, 0);
        in1 = __builtin_amdgcn_raw_buffer_load_b128(rc, (hb * 448 + k1) * 2, 0, 0);
      } else {
        const rsrc_t rx = mkrsrc(a.x, 8 * 448 * 2);
        const int o = lane < 56 ? (2 * vw * 448 + lane * 8) * 2 : 0x7fffff00;
        in0 = __builtin_amdgcn_raw_buffer_load_b128(rx, o, 0, 0);
        in1 = __builtin_amdgcn_raw_buffer_load_b128(rx, o, 896, 0);
      }
      {
        const int stride = isM ? 5376 : 8192;
        const rsrc_t rw = isM ? mkrsrc(a.wmod + e * 32, 448 * 5376 - e * 64) : mkrsrc(a.wq + (e - 84) * 32, 448 * 8192 - (e - 84) * 64);
        const int vo = (lane >> 2) * stride + (lane & 3) * 16;
#pragma unroll
        for (int i = 0; i < 4; ++i) __builtin_amdgcn_raw_ptr_buffer_load_lds(rw, (LAS void*)(L + L_EW + (4 * vw + i) * 1024), 16, vo, (4 * vw + i) * 16 * stride, 0, AUX_NT);
        if (vw < 3) {
#pragma unroll
          for (int i = 0; i < 4; ++i) __builtin_amdgcn_raw_ptr_buffer_load_lds(rw, (LAS void*)(L + L_EW + (4 * vw + 16 + i) * 1024), 16, vo, (4 * vw + 16 + i) * 16 * stride, 0, AUX_NT);
        }
      }
      asm volatile("s_waitcnt lgkmcnt(0)" ::: "memory");
      __builtin_amdgcn_s_barrier();
      STAMP(1);
      const unsigned tag = (__builtin_amdgcn_readfirstlane(ep) + 1u) & 0xffffu;
      if (isM) {
#pragma unroll
        for (int i = 0; i < 4; ++i) { act[0][i] = pk(silu_p(lo16(in0[i])), silu_p(hi16(in0[i]))); act[1][i] = pk(silu_p(lo16(in1[i])), silu_p(hi16(in1[i]))); }
      } else {
#pragma unroll
        for (int rr = 0; rr < 2; ++rr) {
          const v4u in = rr ? in1 : in0;
          float v[8];
#pragma unroll
          for (int i = 0; i < 4; ++i) { v[2 * i] = lo16(in[i]); v[2 * i + 1] = hi16(in[i]); }
          float sm = 0.f;
#pragma unroll
          for (int i = 0; i < 8; ++i) sm += v[i];
          const float mean = wavesum(sm) * (1.f / 448.f);
          float sq = 0.f;
#pragma unroll
          for (int i = 0; i < 8; ++i) { const float d = lane < 56 ? v[i] - mean : 0.f; sq += d * d; }
          const float var = rb(wavesum(sq) * (1.f / 448.f)), mb = rb(mean);
          const float sd = rb(sqrtf(rb(var + 1e-5f)));
          v4u o;
#pragma unroll
          for (int i = 0; i < 4; ++i) o[i] = pk(rb(v[2 * i] - mb) / sd, rb(v[2 * i + 1] - mb) / sd);
          if (lane < 56) *(LAS v4u*)(L + L_XN + ((2 * vw + rr) * 448 + lane * 8) * 2) = o;
        }
        lds_signal(L, F_XN, lane);
        if (!lds_wait(L, F_XN, 4)) return;
        const v4u xn0 = *(LAS v4u*)(L + L_XN + (hb * 448 + k0) * 2), xn1 = *(LAS v4u*)(L + L_XN + (hb * 448 + k1) * 2);
        __builtin_amdgcn_s_setprio(2);
        v4u m[8];
        _Pragma("unroll 1") for (unsigned sp = 0;; ++sp) {
      asm volatile("" ::: "memory");
          const int o0 = WS_H1 + (hb * 2688 + k0) * 4, o1 = WS_H1 + (hb * 2688 + k1) * 4;
          m[0] = ldws(rws, o0); m[1] = ldws(rws, o0 + 16); m[2] = ldws(rws, o0 + 1792); m[3] = ldws(rws, o0 + 1808);
          m[4] = ldws(rws, o1); m[5] = ldws(rws, o1 + 16); m[6] = ldws(rws, o1 + 1792); m[7] = ldws(rws, o1 + 1808);
          unsigned t = 0;
#pragma unroll
          for (int i = 0; i < 8; ++i) t = tagx(t, m[i], tag);
          if (__all((t & 0xffffu) == 0u)) break;
          if (sp > SPIN_G) return;
          __builtin_amdgcn_s_sleep(4);
        }
        STAMP(2);
#pragma unroll
        for (int pi = 0; pi < 2; ++pi) {
          const v4u xn = pi ? xn1 : xn0;
#pragma unroll
          for (int i = 0; i < 4; ++i) {
            const v4u sa = m[4 * pi + (i >> 1)], ca = m[4 * pi + 2 + (i >> 1)];
            const int j = 2 * (i & 1);
            const float h0 = rb(lo16(xn[i]) * rb(1.f + hi16(ca[j]))) + hi16(sa[j]);
            const float h1 = rb(hi16(xn[i]) * rb(1.f + hi16(ca[j + 1]))) + hi16(sa[j + 1]);
            act[pi][i] = pk(h0, h1);
          }
        }
      }
      if (vw == 3) act[1] = v4u{0u, 0u, 0u, 0u};
      waitvm<0>();
      v4u wf[2][2][2];
      read_wfrags(L + L_EW, vw, lane, wf);
      STAMP(3);
      *(LAS f32x4*)(L + L_REDV + vw * 1024 + lane * 16) = gemv_partial(wf, act, lane);
      lds_signal(L, F_REDV, lane);
      if (vw == 0) {
        if (!lds_wait(L, F_REDV, 4)) return;
        const f32x4 o = red4(L + L_REDV, lane);
        const unsigned p01 = pk(o[0], o[1]), p23 = pk(o[2], o[3]);
        const v4u ov = {(p01 << 16) | tag, (p01 & 0xffff0000u) | tag, (p23 << 16) | tag, (p23 & 0xffff0000u) | tag};
        const int off = isM ? WS_H1 + (hb * 2688 + e * 32 + 16 * hf + 4 * g) * 4 : WS_H2 + (hb * 4096 + (e - 84) * 32 + 16 * hf + 4 * g) * 4;
        stws(rws, off, ov);
      }
      __builtin_amdgcn_s_setprio(0);
    } else {
      asm volatile("s_waitcnt lgkmcnt(0)" ::: "memory");
      __builtin_amdgcn_s_barrier();
    }
    STAMP(4);
    __builtin_amdgcn_s_barrier();
    STAMP(5);
    const rsrc_t rv = mkrsrc(a.vc + kvoff, 131072);
    const int vo = (lane >> 3) * 512 + vw * 128 + (lane & 7) * 16;
    v4u v[32];
#pragma unroll
    for (int n = 0; n < 32; ++n) {
      v[n] = __builtin_amdgcn_raw_buffer_load_b128(rv, vo, ((n + rot) & 31) * 4096, AUX_NT);
      __builtin_amdgcn_sched_barrier(0);
      if (LATE_AT > 0 && LATE_AT <= 32 && n + 1 == LATE_AT && vw == 0) lds_signal(L, F_LATE, lane);
      if (n + 1 < 32) waitvm<VDEPTH - 1>();
    }
    STAMP(6);
    if (!lds_wait(L, F_P, 4)) return;
    if (!lds_wait(L, F_SHADOW, 1)) return;
    STAMP(7);
    v4u pf[4];
    {
      const int row = lane & 15, g = lane >> 4, h = (row >> 2) & 1, x = row & 1;
#pragma unroll
      for (int G = 0; G < 4; ++G) pf[G] = *(LAS v4u*)(L + L_P + ((G * 2 + h) * 8 + 2 * g + x) * 16);
    }
    f32x4 acc[8];
#pragma unroll
    for (int j = 0; j < 8; ++j) acc[j] = f32x4{0.f, 0.f, 0.f, 0.f};
#pragma unroll
    for (int G = 0; G < 4; ++G) {
#pragma unroll
      for (int j = 0; j < 8; ++j) {
        v4u f;
#pragma unroll
        for (int q = 0; q < 4; ++q) f[q] = __builtin_amdgcn_perm(v[8 * G + 2 * q + 1][j >> 1], v[8 * G + 2 * q][j >> 1], (j & 1) ? 0x07060302u : 0x05040100u);
        acc[j] = mfma(pf[G], f, acc[j]);
      }
    }
    float o[8];
#pragma unroll
    for (int j = 0; j < 8; ++j) o[j] = acc[j][0] + dpp<ROR8>(acc[j][1]);
    const v4u ov = nosent(v4u{pk(o[0], o[1]), pk(o[2], o[3]), pk(o[4], o[5]), pk(o[6], o[7])});
    if ((lane & 0x28) == 0) {
      const int h = 2 * kvh + (lane >> 4), c = lane & 7, ks = 2 * vw + (c >> 2);
      stws(rws, WS_H3 + ((h * 8 + ks) * 2 + (b >> 2)) * 1024 + (4 * (b & 3) + s) * 64 + (c & 3) * 16, ov);
    }
    if (LATE_AT > 32 && vw == 0) lds_signal(L, F_LATE, lane);
    STAMP(8);
    STAMP_OUT();
    return;
  }

  const int kw = w - 4;
  unsigned tag;
  {
    const rsrc_t rk = mkrsrc(a.kc + kvoff, 131072);
    const int vo = ((lane >> 5) * 4 + ((lane >> 2) & 3)) * 512 + (((lane >> 4) & 1) * 4 + (lane & 3)) * 16;
    v4u k[32];
    unsigned ep = 0;
#pragma unroll
    for (int n = 0; n < 32; ++n) {
      k[n] = __builtin_amdgcn_raw_buffer_load_b128(rk, vo + (n & 3) * 128, ((4 * (n >> 2) + kw + rot) & 31) * 4096, AUX_NT);
      __builtin_amdgcn_sched_barrier(0);
      if (n == KDEPTH - 1) {
        ep = ldws1(rws, WS_EPOCH);
        const v4u sent = {~0u, ~0u, ~0u, ~0u};
        const int co = WS_H3 + bid * 9216 + lane * 16;
        stws(rws, co + kw * 1024, sent); stws(rws, co + (kw + 4) * 1024, sent);
        if (kw == 0) stws(rws, co + 8192, sent);
        __builtin_amdgcn_s_barrier();
        __builtin_amdgcn_sched_barrier(0);
      }
      if (n >= KDEPTH - 1 && n + 1 < 32) waitvm<KDEPTH - 1>();
    }
    STAMP(1);
    __builtin_amdgcn_s_barrier();
    STAMP(2);
    tag = (__builtin_amdgcn_readfirstlane(ep) + 1u) & 0xffffu;
    __builtin_amdgcn_s_setprio(2);
    if (kw == 0) {
      _Pragma("unroll 1") for (unsigned sp = 0;; ++sp) {
      asm volatile("" ::: "memory");
        const int o = WS_H2 + (b * 4096 + kvh * 512 + lane * 8) * 4;
        const v4u q0 = ldws(rws, o), q1 = ldws(rws, o + 16);
        if (__all((tagx(tagx(0u, q0, tag), q1, tag) & 0xffffu) == 0u)) {
          *(LAS v4u*)(L + L_Q + lane * 16) = v4u{__builtin_amdgcn_perm(q0.y, q0.x, 0x07060302u), __builtin_amdgcn_perm(q0.w, q0.z, 0x07060302u),
                                                 __builtin_amdgcn_perm(q1.y, q1.x, 0x07060302u), __builtin_amdgcn_perm(q1.w, q1.z, 0x07060302u)};
          break;
        }
        if (sp > SPIN_G) return;
        __builtin_amdgcn_s_sleep(4);
      }
      lds_signal(L, F_Q, lane);
    }
    if (!lds_wait(L, F_Q, 1)) return;
    STAMP(3);
    v4u qf[4];
    {
      const int row = lane & 15, g1r = row >> 3, h = (row >> 2) & 1, bq = row & 3, g1 = lane >> 5, g0 = (lane >> 4) & 1;
#pragma unroll
      for (int j = 0; j < 4; ++j) {
        const v4u t = *(LAS v4u*)(L + L_Q + (h * 256 + 64 * j + 8 * (4 * g0 + bq)) * 2);
        qf[j] = g1 == g1r ? t : v4u{0u, 0u, 0u, 0u};
      }
    }
    float S[2];
    const int bb = lane & 3;
#pragma unroll
    for (int i = 0; i < 8; ++i) {
      f32x4 d = {0.f, 0.f, 0.f, 0.f};
#pragma unroll
      for (int j = 0; j < 4; ++j) d = mfma(qf[j], k[4 * i + j], d);
      const float sc = (dpp<0x00>(d[0]) + dpp<0x55>(d[1])) + (dpp<0xAA>(d[2]) + dpp<0xFF>(d[3]));
      if ((i & 3) == 0) S[i >> 2] = sc; else S[i >> 2] = bb == (i & 3) ? sc : S[i >> 2];
    }
    STAMP(4);
    constexpr float LOG2E = 1.4426950408889634f;
    const float s0 = rb(S[0]) * 0.0625f, s1 = rb(S[1]) * 0.0625f;
    const float mw = max32(rowmax(fmaxf(s0, s1)));
    float lw = rowsum(__builtin_amdgcn_exp2f((s0 - mw) * LOG2E) + __builtin_amdgcn_exp2f((s1 - mw) * LOG2E));
    lw = sum32swap(lw, lw);
    const int hh = (lane >> 4) & 1;
    if ((lane & 0x2f) == 0) *(LAS f32x2*)(L + L_ML + (kw * 2 + hh) * 8) = f32x2{mw, lw};
    lds_signal(L, F_ML, lane);
    if (kw == 0) {
      if (!lds_wait(L, F_ML, 4)) return;
      const int h = lane & 1;
      float m = -INFINITY, l = 0.f;
      f32x2 t[4];
#pragma unroll
      for (int i = 0; i < 4; ++i) { t[i] = *(LAS f32x2*)(L + L_ML + (i * 2 + h) * 8); m = fmaxf(m, t[i].x); }
#pragma unroll
      for (int i = 0; i < 4; ++i) l += t[i].y * __builtin_amdgcn_exp2f((t[i].x - m) * LOG2E);
      const int quad = WS_STATS + (b * 8 + kvh) * 64;
      if (lane < 2) __builtin_amdgcn_raw_buffer_store_b64(v2u{asu(l), (asu(m) & 0xffff0000u) | tag}, rws, quad + s * 16 + h * 8, 0, AUX_SC1);
      STAMP(5);
      v2u pe[4];
      _Pragma("unroll 1") for (unsigned sp = 0;; ++sp) {
      asm volatile("" ::: "memory");
        unsigned t2 = 0;
#pragma unroll
        for (int i = 0; i < 4; ++i) { pe[i] = __builtin_amdgcn_raw_buffer_load_b64(rws, quad + i * 16 + h * 8, 0, AUX_SC1); t2 |= pe[i].y ^ tag; }
        if (__all((t2 & 0xffffu) == 0u)) break;
        if (sp > SPIN_G) return;
        __builtin_amdgcn_s_sleep(4);
      }
      float M = -INFINITY, Lg = 0.f;
#pragma unroll
      for (int i = 0; i < 4; ++i) M = fmaxf(M, hi16(pe[i].y));
#pragma unroll
      for (int i = 0; i < 4; ++i) Lg += asf(pe[i].x) * __builtin_amdgcn_exp2f((hi16(pe[i].y) - M) * LOG2E);
      if (lane < 2) *(LAS f32x2*)(L + L_ML + 64 + h * 8) = f32x2{M, 1.f / Lg};
      lds_signal(L, F_GLOB, lane);
    }
    if (!lds_wait(L, F_GLOB, 1)) return;
    STAMP(6);
    const f32x2 g = *(LAS f32x2*)(L + L_ML + 64 + hh * 8);
    const unsigned pp = pk(__builtin_amdgcn_exp2f((s0 - g.x) * LOG2E) * g.y, __builtin_amdgcn_exp2f((s1 - g.x) * LOG2E) * g.y);
    {
      const int ks8 = 4 * (lane >> 5) + ((lane >> 2) & 3), iv = 4 * (bb & 1) + kw, G0 = bb >> 1;
      *(LAS u16*)(L + L_P + ((G0 * 2 + hh) * 8 + ks8) * 16 + iv * 2) = (u16)pp;
      *(LAS u16*)(L + L_P + (((G0 + 2) * 2 + hh) * 8 + ks8) * 16 + iv * 2) = (u16)(pp >> 16);
    }
    lds_signal(L, F_P, lane);
    if (kw == 1) {
      _Pragma("unroll 1") for (unsigned sp = 0;; ++sp) {
      asm volatile("" ::: "memory");
        unsigned t2 = 0;
#pragma unroll
        for (int i = 0; i < 4; ++i) { const v4u t = ldws(rws, WS_STATS + i * 1024 + lane * 16); t2 |= (t.y ^ tag) | (t.w ^ tag); }
        if (__all((t2 & 0xffffu) == 0u)) break;
        if (sp > SPIN_G) return;
        __builtin_amdgcn_s_sleep(4);
      }
      lds_signal(L, F_SHADOW, lane);
      if (bid == 0 && lane == 0) __builtin_amdgcn_raw_buffer_store_b32(ep + 1u, rws, WS_EPOCH, 0, AUX_SC1);
    }
    STAMP(7);
  }

  const int fq = (7 - (r >> 2)) * 8 + xc;
  if (fq >= 56) {
    const int t = ((fq - 56) * 4 + (r & 3)) * 4 + kw;
    if (t < 112) {
      const int tb = t / 14, m = t - tb * 14;
      const int col = colchunk(m, lane >> 4) * 8 + (lane & 7);
      const float xv = lo16(a.x[tb * 448 + col]);
      unsigned ga, cb;
      _Pragma("unroll 1") for (unsigned sp = 0;; ++sp) {
      asm volatile("" ::: "memory");
        ga = ldws1(rws, WS_H1 + (tb * 2688 + 2 * 448 + col) * 4); cb = ldws1(rws, WS_H1 + (tb * 2688 + 4 * 448 + col) * 4);
        if (__all((((ga ^ tag) | (cb ^ tag)) & 0xffffu) == 0u)) break;
        if (sp > SPIN_G) return;
        __builtin_amdgcn_s_sleep(4);
      }
      const float gaf = hi16(ga), cc = rb(1.f + hi16(cb));
      if (!lds_wait(L, F_SHADOW, 1)) return;
      STAMP(8);
      _Pragma("unroll 1") for (unsigned sp = 0;; ++sp) {
      asm volatile("" ::: "memory");
        v4u y[4];
#pragma unroll
        for (int i = 0; i < 4; ++i) y[i] = ldws(rws, WS_H4 + t * 4096 + i * 1024 + lane * 16);
        const bool ok = __all(smax(smax(smax(smax(0u, y[0]), y[1]), y[2]), y[3]) != ~0u);
        if (ok || sp == 0) {
          const f32x4 sv = (__builtin_bit_cast(f32x4, y[0]) + __builtin_bit_cast(f32x4, y[1])) + (__builtin_bit_cast(f32x4, y[2]) + __builtin_bit_cast(f32x4, y[3]));
          float u = sum16swap(sum32swap(sv[0], sv[1]), sum32swap(sv[2], sv[3]));
          u += dpp<ROR8>(u);
          const float x1 = rb(xv + rb(gaf * rb(u)));
          const unsigned o = min(pk(x1, x1 * cc), 0xfffffffeu);
          if (ok) {
            if ((lane & 8) == 0) __builtin_amdgcn_raw_buffer_store_b32(o, rws, WS_H5 + (tb * 448 + m * 32 + (lane >> 4) * 8 + (lane & 7)) * 4, 0, AUX_SC1);
            break;
          }
          asm volatile("" ::"v"(o));
        }
        if (sp > SPIN_G) return;
      }
      STAMP(9);
    }
    STAMP_OUT();
    return;
  }

  if (LATE_AT > 0) { if (!lds_wait(L, F_LATE, 1)) return; }

  if (fq >= 28) {
    const int wt = (fq - 28) * 4 + (r & 3), h = wt / 7, cb64 = wt - h * 7, khalf = kw >> 1, rt = kw & 1;
    {
      const rsrc_t rw = mkrsrc(a.wo + (size_t)h * 256 * 448 + cb64 * 64, 256 * 896 - cb64 * 128);
      const int vo = (lane >> 3) * 896 + (lane & 7) * 16;
#pragma unroll
      for (int i = 0; i < 8; ++i) __builtin_amdgcn_raw_ptr_buffer_load_lds(rw, (LAS void*)(L + L_LW + (8 * kw + i) * 1024), 16, vo, (8 * kw + i) * 8 * 896, 0, AUX_NT);
    }
    const int t = wt * 2 + (kw & 1), tb = t / 28, tile = t - tb * 28;
    const int rho = lane >> 4, ocol = 16 * tile + 4 * (lane & 3) + (((rho & 1) << 1) | (rho >> 1));
    unsigned gb;
    _Pragma("unroll 1") for (unsigned sp = 0;; ++sp) {
      asm volatile("" ::: "memory");
      gb = ldws1(rws, WS_H1 + (tb * 2688 + 5 * 448 + ocol) * 4);
      if (__all(((gb ^ tag) & 0xffffu) == 0u)) break;
      if (sp > SPIN_G) return;
      __builtin_amdgcn_s_sleep(4);
    }
    waitvm<0>();
    lds_signal(L, F_REDV, lane);
    const int e = 2 * ((r >> 1) * 8 + xc) + (r & 1);
    if (!lds_wait(L, F_REDV, (e < 212 ? 4u : 0u) + 4u)) return;
    v4u wf[4][4];
    {
      const int g = lane >> 4, q = (lane >> 2) & 3, p = lane & 3;
#pragma unroll
      for (int i = 0; i < 4; ++i)
#pragma unroll
        for (int ct = 0; ct < 4; ++ct) {
          LAS unsigned char* bp = L + L_LW + (128 * khalf + 32 * i + 8 * g + q) * 128 + ct * 32 + p * 8;
          wf[i][ct] = trfrag(bp, bp + 512);
        }
    }
    if (!lds_wait(L, F_SHADOW, 1)) return;
    STAMP(8);
    _Pragma("unroll 1") for (unsigned sp = 0;; ++sp) {
      asm volatile("" ::: "memory");
      v4u of[4];
#pragma unroll
      for (int i = 0; i < 4; ++i) of[i] = ldws(rws, WS_H3 + ((h * 8 + 4 * khalf + i) * 2 + rt) * 1024 + (lane & 15) * 64 + (lane >> 4) * 16);
      const bool ok = __all(smax(smax(smax(smax(0u, of[0]), of[1]), of[2]), of[3]) != ~0u);
      if (ok || sp == 0) {
        f32x4 d[4];
#pragma unroll
        for (int ct = 0; ct < 4; ++ct) d[ct] = mfma(of[0], wf[0][ct], f32x4{0.f, 0.f, 0.f, 0.f});
#pragma unroll
        for (int i = 1; i < 4; ++i)
#pragma unroll
          for (int ct = 0; ct < 4; ++ct) d[ct] = mfma(of[i], wf[i][ct], d[ct]);
        v4u o;
#pragma unroll
        for (int ct = 0; ct < 4; ++ct) o[ct] = asu((d[ct][0] + d[ct][1]) + (d[ct][2] + d[ct][3]));
        o = nosent(o);
        if (ok) {
          const int c = lane & 15, yb = 4 * rt + (lane >> 4);
          stws(rws, WS_H4 + ((yb * 14 + 2 * cb64 + (c >> 3)) * 32 + 2 * h + khalf) * 128 + (c & 7) * 16, o);
          break;
        }
        asm volatile("" ::"v"(o));
      }
      if (sp > SPIN_G) return;
    }
    STAMP(9);
    if (kw >= 2) { STAMP_OUT(); return; }
    const int cc8 = ocol >> 3, mm = 2 * (cc8 >> 3) + (cc8 & 1), ct2 = (cc8 >> 1) & 3, rh = ((ct2 & 1) << 1) | (ct2 >> 1);
    unsigned xz;
    _Pragma("unroll 1") for (unsigned sp = 0;; ++sp) {
      asm volatile("" ::: "memory");
      xz = ldws1(rws, WS_H5 + (tb * 448 + mm * 32 + rh * 8 + (ocol & 7)) * 4);
      if (__all(xz != ~0u)) break;
      if (sp > SPIN_G) return;
    }
    STAMP(10);
    const float x1 = lo16(xz), gbf = hi16(gb);
    _Pragma("unroll 1") for (unsigned sp = 0;; ++sp) {
      asm volatile("" ::: "memory");
      v4u y[7];
#pragma unroll
      for (int i = 0; i < 7; ++i) y[i] = ldws(rws, WS_H6 + t * 7168 + i * 1024 + lane * 16);
      unsigned mx = 0;
#pragma unroll
      for (int i = 0; i < 7; ++i) mx = smax(mx, y[i]);
      const bool ok = __all(mx != ~0u);
      if (ok || sp == 0) {
        f32x4 sv = ((__builtin_bit_cast(f32x4, y[0]) + __builtin_bit_cast(f32x4, y[1])) + (__builtin_bit_cast(f32x4, y[2]) + __builtin_bit_cast(f32x4, y[3]))) +
                   ((__builtin_bit_cast(f32x4, y[4]) + __builtin_bit_cast(f32x4, y[5])) + __builtin_bit_cast(f32x4, y[6]));
        float u = sum16swap(sum32swap(sv[0], sv[1]), sum32swap(sv[2], sv[3]));
        u += dpp<ROR8>(u);
        u += dpp<ROR4>(u);
        const unsigned o = pk(x1 + rb(gbf * rb(u)), 0.f);
        if (ok) {
          if ((lane & 12) == 0) a.out[tb * 448 + ocol] = (u16)o;
          break;
        }
        asm volatile("" ::"v"(o));
      }
      if (sp > SPIN_G) return;
    }
    STAMP(11);
    STAMP_OUT();
    return;
  }

  {
    const int f = fq * 4 + (r & 3);
    const int col = lane & 15, g = lane >> 4, hb = col & 7, hf = col >> 3;
    const int P1 = kw == 3 ? 3 : kw + 4;
    {
      const u16* wlo = a.w1 < a.w3 ? a.w1 : a.w3;
      const size_t d1 = (size_t)(a.w1 - wlo) * 2, d3 = (size_t)(a.w3 - wlo) * 2;
      const int prow = lane >> 2, piece = lane & 3;
#pragma unroll
      for (int pi = 0; pi < 2; ++pi) {
        if (pi == 1 && kw == 3) break;
        const int P = kw + 4 * pi;
#pragma unroll
        for (int i = 0; i < 4; ++i) {
          const int ks = 2 * P + (i >> 1), rho = 2 * (i & 1) + (prow >> 3);
          const int row = 8 * colchunk(ks, rho) + (prow & 7);
          const unsigned char* src = (const unsigned char*)wlo + (piece < 2 ? d1 : d3) + (size_t)(row * 1792 + f * 16) * 2 + (piece & 1) * 16;
          __builtin_amdgcn_global_load_lds((const __attribute__((address_space(1))) void*)src, (LAS void*)(L + L_LW + (4 * P + i) * 1024), 16, 0, AUX_NT);
        }
      }
      const rsrc_t r2 = mkrsrc(a.w2 + (size_t)f * 16 * 448, 14336);
#pragma unroll
      for (int i = 0; i < 4; ++i)
        if (4 * i + kw < 14) __builtin_amdgcn_raw_ptr_buffer_load_lds(r2, (LAS void*)(L + L_W2 + (4 * i + kw) * 1024), 16, lane * 16, (4 * i + kw) * 1024, 0, AUX_NT);
    }
    const int c0 = 8 * colchunk(2 * kw + hf, g), c1 = 8 * colchunk(2 * P1 + hf, g);
    v4u ccf[2], sbf[2];
    {
      v4u m[8];
      _Pragma("unroll 1") for (unsigned sp = 0;; ++sp) {
      asm volatile("" ::: "memory");
        const int o0 = WS_H1 + (hb * 2688 + 3 * 448 + c0) * 4, o1 = WS_H1 + (hb * 2688 + 3 * 448 + c1) * 4;
        m[0] = ldws(rws, o0); m[1] = ldws(rws, o0 + 16); m[2] = ldws(rws, o0 + 1792); m[3] = ldws(rws, o0 + 1808);
        m[4] = ldws(rws, o1); m[5] = ldws(rws, o1 + 16); m[6] = ldws(rws, o1 + 1792); m[7] = ldws(rws, o1 + 1808);
        unsigned t = 0;
#pragma unroll
        for (int i = 0; i < 8; ++i) t = tagx(t, m[i], tag);
        if (__all((t & 0xffffu) == 0u)) break;
        if (sp > SPIN_G) return;
        __builtin_amdgcn_s_sleep(4);
      }
#pragma unroll
      for (int pi = 0; pi < 2; ++pi)
#pragma unroll
        for (int i = 0; i < 4; ++i) {
          const v4u sb = m[4 * pi + (i >> 1)], cb = m[4 * pi + 2 + (i >> 1)];
          const int j = 2 * (i & 1);
          sbf[pi][i] = __builtin_amdgcn_perm(sb[j + 1], sb[j], 0x07060302u);
          ccf[pi][i] = pk(1.f + hi16(cb[j]), 1.f + hi16(cb[j + 1]));
        }
      if (kw == 3) { ccf[1] = v4u{0u, 0u, 0u, 0u}; sbf[1] = v4u{0u, 0u, 0u, 0u}; }
    }
    waitvm<0>();
    v4u wf[2][2][2];
    read_wfrags(L + L_LW, kw, lane, wf);
    *(LAS f32x4*)(L + L_REDK + kw * 1024 + lane * 16) = gemv_partial(wf, ccf, lane);
    *(LAS f32x4*)(L + L_REDK + 4096 + kw * 1024 + lane * 16) = gemv_partial(wf, sbf, lane);
    asm volatile("s_waitcnt lgkmcnt(0)" ::: "memory");
    __builtin_amdgcn_s_barrier();
    const f32x4 T1 = red4(L + L_REDK, lane), T2 = red4(L + L_REDK + 4096, lane);
    v4u w2f[4];
    {
      const int q = (lane >> 2) & 3, p = lane & 3;
#pragma unroll
      for (int i = 0; i < 4; ++i) {
        const int tp = min(4 * i + kw, 13);
        LAS unsigned char* bp = L + L_W2 + (4 * g + q) * 896 + tp * 64 + p * 8;
        w2f[i] = trfrag(bp, bp + 32);
      }
    }
    asm volatile("s_waitcnt lgkmcnt(0)" ::: "memory");
    __builtin_amdgcn_s_barrier();
    if (!lds_wait(L, F_SHADOW, 1)) return;
    STAMP(8);
    const int z0 = WS_H5 + (hb * 448 + (2 * kw + hf) * 32 + g * 8) * 4, z1 = WS_H5 + (hb * 448 + (2 * P1 + hf) * 32 + g * 8) * 4;
    int pass = 0;
    asm volatile("" : "+s"(pass));
    _Pragma("unroll 1") for (; pass < 2; ++pass) {
      v4u zz[4];
      _Pragma("unroll 1") for (unsigned sp = 0;; ++sp) {
      asm volatile("" ::: "memory");
        zz[0] = ldws(rws, z0); zz[1] = ldws(rws, z0 + 16); zz[2] = ldws(rws, z1); zz[3] = ldws(rws, z1 + 16);
        if (pass == 0 || __all(smax(smax(smax(smax(0u, zz[0]), zz[1]), zz[2]), zz[3]) != ~0u)) break;
        if (sp > SPIN_G) return;
      }
      STAMP(9);
      v4u zf[2], xf[2];
#pragma unroll
      for (int pi = 0; pi < 2; ++pi)
#pragma unroll
        for (int i = 0; i < 4; ++i) {
          const v4u t = zz[2 * pi + (i >> 1)];
          const int j = 2 * (i & 1);
          zf[pi][i] = __builtin_amdgcn_perm(t[j + 1], t[j], 0x07060302u);
          xf[pi][i] = __builtin_amdgcn_perm(t[j + 1], t[j], 0x05040100u);
        }
      if (kw == 3) { zf[1] = v4u{0u, 0u, 0u, 0u}; xf[1] = v4u{0u, 0u, 0u, 0u}; }
      const f32x4 part = gemv_partial(wf, zf, lane);
      const v4u ones = {0x3f803f80u, 0x3f803f80u, 0x3f803f80u, 0x3f803f80u};
      f32x4 s1 = mfma(ones, xf[0], f32x4{0.f, 0.f, 0.f, 0.f}), s2 = mfma(xf[0], xf[0], f32x4{0.f, 0.f, 0.f, 0.f});
      s1 = mfma(ones, xf[1], s1); s2 = mfma(xf[1], xf[1], s2);
      *(LAS f32x4*)(L + L_REDK + kw * 1024 + lane * 16) = part;
      {
        const int cr = col & 3;
        const float dg = cr == 0 ? s2[0] : cr == 1 ? s2[1] : cr == 2 ? s2[2] : s2[3];
        if ((col >> 2) == g) *(LAS f32x2*)(L + L_STK + (hb * 8 + kw * 2 + hf) * 8) = f32x2{s1[0], dg};
      }
      asm volatile("s_waitcnt lgkmcnt(0)" ::: "memory");
      __builtin_amdgcn_s_barrier();
      const f32x4 Z = red4(L + L_REDK, lane);
      LAS f32x4* sp4 = (LAS f32x4*)(L + L_STK + hb * 64);
      const f32x4 t0 = (sp4[0] + sp4[1]) + (sp4[2] + sp4[3]);
      const float mu = (t0[0] + t0[2]) * (1.f / 448.f), var = fmaxf((t0[1] + t0[3]) * (1.f / 448.f) - mu * mu, 0.f);
      const float rs = __builtin_amdgcn_rsqf(var + 1e-5f);
      float hv[4];
#pragma unroll
      for (int i = 0; i < 4; ++i) {
        const float val = rb(fmaf(rs, fmaf(-mu, T1[i], Z[i]), T2[i])), oth = dpp<ROR8>(val);
        const float gt = hf ? oth : val, up = hf ? val : oth;
        hv[i] = rb(silu(gt)) * up;
      }
      const unsigned h01 = pk(hv[0], hv[1]), h23 = pk(hv[2], hv[3]);
      const v4u hf4 = {hf ? 0u : h01, hf ? 0u : h23, hf ? h01 : 0u, hf ? h23 : 0u};
#pragma unroll
      for (int i = 0; i < 4; ++i) {
        const v4u o = nosent(__builtin_bit_cast(v4u, mfma(w2f[i], hf4, f32x4{0.f, 0.f, 0.f, 0.f})));
        const int tp = 4 * i + kw;
        if (pass == 1 && tp < 14) stws(rws, WS_H6 + ((hb * 28 + 2 * tp + hf) * 112 + f) * 64 + g * 16, o);
        else asm volatile("" ::"v"(o));
      }
      if (pass == 0) __builtin_amdgcn_s_barrier();
    }
    STAMP(10);
    STAMP_OUT();
  }
}

extern "C" void kernel_launch(void* const* d_in, const int* in_sizes, int n_in, void* d_out, int out_size, void* d_ws, size_t ws_size, hipStream_t stream) {
  Args a;
  a.x = (const u16*)d_in[0]; a.kc = (const u16*)d_in[1]; a.vc = (const u16*)d_in[2]; a.wq = (const u16*)d_in[3]; a.wo = (const u16*)d_in[4];
  a.w1 = (const u16*)d_in[6]; a.w3 = (const u16*)d_in[7]; a.w2 = (const u16*)d_in[8]; a.c = (const u16*)d_in[9]; a.wmod = (const u16*)d_in[10];
  a.out = (u16*)d_out; a.ws = (unsigned char*)d_ws;
  (void)hipFuncSetAttribute((const void*)fused_block, hipFuncAttributeMaxDynamicSharedMemorySize, L_BYTES);
  fused_block<<<256, 512, L_BYTES, stream>>>(a);
}

static int wave_class(int bid, int w) {
  const int xc = bid & 7, r = bid >> 3;
  if (w < 4) { const int e = 2 * ((r >> 1) * 8 + xc) + (r & 1); return e < 28 ? 0 : e < 84 ? 1 : e < 212 ? 2 : 3; }
  const int fq = (7 - (r >> 2)) * 8 + xc;
  return fq >= 56 ? 4 : fq >= 28 ? 5 : 6;
}
```

```cpp
#include <hip/hip_runtime.h>
#include <hip/hip_bf16.h>
#include <stdint.h>

#define STAMPS 0
#define DEPTH_A 6
#define DEPTH_B 6
#define NSW 6
#define NB_Q 24
#define CUT_M 5
#define DUP_M 0
#define KQ1 2
#define ABL 0
#define QT0 640
#define QDT 50
#define WS_AT_END 1
#define LATE_W 0
#define LATE_F 0
constexpr int NBMAX = 40, NBMIN = NB_Q;

typedef unsigned short u16;
typedef unsigned long long u64;
typedef unsigned v4u __attribute__((ext_vector_type(4)));
typedef unsigned v2u __attribute__((ext_vector_type(2)));
typedef float f32x4 __attribute__((ext_vector_type(4)));
typedef float f32x2 __attribute__((ext_vector_type(2)));
typedef __bf16 bf16x8 __attribute__((ext_vector_type(8)));
typedef __bf16 bf16x2 __attribute__((ext_vector_type(2)));
typedef short s16x4 __attribute__((ext_vector_type(4)));
#define LAS __attribute__((address_space(3)))
#define DEV __device__ __forceinline__
typedef __amdgpu_buffer_rsrc_t rsrc_t;

constexpr int AUX_NT = 2, AUX_SC1 = 16;
constexpr int WS_EPOCH = 0, WS_READY = 2048, WS_STATS = 4096, WS_H1 = 8192, WS_H2 = WS_H1 + 42 * 2048;
constexpr int WS_H3 = WS_H2 + 8 * 4096 * 4, WS_H4 = WS_H3 + 262144, WS_H5 = WS_H4 + 458752, WS_H6 = WS_H5 + 8 * 14336;
constexpr int WS_END = WS_H6 + 8 * 28672;
constexpr int CLR_PER_WG = 4224;
constexpr int WS_CLR_END = WS_H3 + 256 * CLR_PER_WG;
static_assert(WS_END <= WS_CLR_END, "clear partition");
constexpr int WS_STAMP = 4 << 20;
constexpr int WS_BYTES = STAMPS ? (8 << 20) : WS_CLR_END;
constexpr int L_EW = 0;
constexpr int L_XN = 57344;
constexpr int L_RED = 65536;
constexpr int L_STK = L_RED + 16384;
constexpr int L_Q = L_STK + 256;
constexpr int L_P = L_Q + 2048;
constexpr int L_ML = L_P + 1280;
constexpr int L_FLAG = L_ML + 64;
constexpr int L_TCH = L_FLAG + 64;
constexpr int L_BYTES = L_TCH + 256;
enum { F_XN = 0, F_RED = 1, F_L = 2, F_SHADOW = 3, F_WLD = 4, F_P = 5, F_ML = 6, F_LATE = 7, F_PUB = 8, F_GO = 9 };

constexpr unsigned SPIN_G = 1u << 16, SPIN_L = 1u << 22;

struct Args { const u16 *x, *kc, *vc, *wq, *wo, *w1, *w3, *w2, *c, *wmod; u16* out; unsigned char* ws; };
struct Roles { unsigned v[256]; };

#define EXIT() __builtin_amdgcn_endpgm()
#define POLL_BEGIN(cap) { unsigned sp = (cap); bool ok; _Pragma("unroll 1") do { asm volatile("" ::: "memory");
#define POLL_END } while (!ok && --sp); asm volatile("" ::: "memory"); }
#define SB() __builtin_amdgcn_sched_barrier(0)

DEV rsrc_t mkrsrc(const void* p, unsigned bytes) { return __builtin_amdgcn_make_buffer_rsrc((void*)p, 0, bytes, 0x00020000); }
DEV f32x4 mfma(v4u a, v4u b, f32x4 c) { return __builtin_amdgcn_mfma_f32_16x16x32_bf16(__builtin_bit_cast(bf16x8, a), __builtin_bit_cast(bf16x8, b), c, 0, 0, 0); }
DEV unsigned pk(float lo, float hi) { f32x2 v = {lo, hi}; return __builtin_bit_cast(unsigned, __builtin_convertvector(v, bf16x2)); }
DEV float asf(unsigned u) { return __builtin_bit_cast(float, u); }
DEV unsigned asu(float f) { return __builtin_bit_cast(unsigned, f); }
DEV f32x4 asf4(v4u u) { return __builtin_bit_cast(f32x4, u); }
DEV float lo16(unsigned u) { return asf(u << 16); }
DEV float hi16(unsigned u) { return asf(u & 0xffff0000u); }
DEV float rb(float f) { return lo16(pk(f, f)); }
DEV unsigned hi2(unsigned lo, unsigned hi) { return __builtin_amdgcn_perm(hi, lo, 0x07060302u); }
DEV unsigned lo2(unsigned lo, unsigned hi) { return __builtin_amdgcn_perm(hi, lo, 0x05040100u); }
template <int CTRL> DEV unsigned dppu(unsigned x) { return (unsigned)__builtin_amdgcn_mov_dpp((int)x, CTRL, 0xf, 0xf, true); }
template <int CTRL> DEV float dpp(float x) { return asf(dppu<CTRL>(asu(x))); }
constexpr int ROR1 = 0x121, ROR2 = 0x122, ROR4 = 0x124, ROR8 = 0x128, HMIRROR = 0x141;
template <int BANKS> DEV unsigned half_from(unsigned old, unsigned src) { return (unsigned)__builtin_amdgcn_update_dpp((int)old, (int)src, ROR8, 0xf, BANKS, false); }
DEV float sum32swap(float a, float b) { auto r = __builtin_amdgcn_permlane32_swap(asu(a), asu(b), false, false); return asf(r[0]) + asf(r[1]); }
DEV float sum16swap(float a, float b) { auto r = __builtin_amdgcn_permlane16_swap(asu(a), asu(b), false, false); return asf(r[0]) + asf(r[1]); }
DEV float max32(float a) { auto r = __builtin_amdgcn_permlane32_swap(asu(a), asu(a), false, false); return fmaxf(asf(r[0]), asf(r[1])); }
DEV float rowsum(float v) { v += dpp<ROR8>(v); v += dpp<ROR4>(v); v += dpp<ROR2>(v); v += dpp<ROR1>(v); return v; }
DEV float rowmax(float v) { v = fmaxf(v, dpp<ROR8>(v)); v = fmaxf(v, dpp<ROR4>(v)); v = fmaxf(v, dpp<ROR2>(v)); v = fmaxf(v, dpp<ROR1>(v)); return v; }
DEV float wavesum(float v) { v = rowsum(v); v = sum16swap(v, v); return sum32swap(v, v); }
DEV unsigned umax3(unsigned a, unsigned b, unsigned c) { return max(max(a, b), c); }
DEV unsigned smax(unsigned acc, v4u v) { return umax3(umax3(acc, v.x, v.y), v.z, v.w); }
DEV unsigned tagx(unsigned acc, v4u v, unsigned tag) { return acc | (v.x ^ tag) | (v.y ^ tag) | (v.z ^ tag) | (v.w ^ tag); }
DEV unsigned nosent(unsigned v) { return min(v, 0xfffffffeu); }
DEV v4u nosent(v4u v) { return v4u{nosent(v.x), nosent(v.y), nosent(v.z), nosent(v.w)}; }
template <int N> DEV void waitvm() { asm volatile("s_waitcnt vmcnt(%0)" ::"i"(N) : "memory"); }
constexpr float LOG2E = 1.4426950408889634f;
DEV float ex2(float v) { return __builtin_amdgcn_exp2f(v); }
DEV float silu_p(float v) { return v / (1.f + ex2(v * -LOG2E)); }
DEV float silu(float v) { return v * __builtin_amdgcn_rcpf(1.f + ex2(v * -LOG2E)); }
constexpr f32x4 ZERO4 = {0.f, 0.f, 0.f, 0.f};

DEV v4u ldws(rsrc_t rs, int off) { return __builtin_amdgcn_raw_buffer_load_b128(rs, off, 0, AUX_SC1); }
DEV v2u ldws2(rsrc_t rs, int off) { return __builtin_amdgcn_raw_buffer_load_b64(rs, off, 0, AUX_SC1); }
DEV unsigned ldws1(rsrc_t rs, int off) { return __builtin_amdgcn_raw_buffer_load_b32(rs, off, 0, AUX_SC1); }
DEV void stws(rsrc_t rs, int off, v4u v) { __builtin_amdgcn_raw_buffer_store_b128(v, rs, off, 0, AUX_SC1); }
DEV void stws2(rsrc_t rs, int off, v2u v) { __builtin_amdgcn_raw_buffer_store_b64(v, rs, off, 0, AUX_SC1); }
DEV void dma16(const void* src, unsigned lds) {
  unsigned keep;
  asm volatile("s_mov_b32 %0, m0\n\ts_mov_b32 m0, %2\n\ts_nop 0\n\tglobal_load_lds_dwordx4 %1, off nt\n\ts_mov_b32 m0, %0" : "=&s"(keep) : "v"(src), "s"(lds) : "memory");
}

DEV void dma16_sc1(const void* src, unsigned lds) {
  unsigned keep;
  asm volatile("s_mov_b32 %0, m0\n\ts_mov_b32 m0, %2\n\ts_nop 0\n\tglobal_load_lds_dwordx4 %1, off sc1\n\ts_mov_b32 m0, %0" : "=&s"(keep) : "v"(src), "s"(lds) : "memory");
}

DEV void lds_signal(LAS unsigned char* L, int flag, int lane) {
  if (lane == 0) asm volatile("ds_add_u32 %0, %1" ::"v"(L_FLAG + 4 * flag), "v"(1u) : "memory");
}
DEV unsigned lds_peek(LAS unsigned char* L, int flag) {
  return __builtin_amdgcn_readfirstlane(__hip_atomic_load((LAS unsigned*)(L + L_FLAG) + flag, __ATOMIC_RELAXED, __HIP_MEMORY_SCOPE_WORKGROUP));
}
template <bool SLEEP = true> DEV void lds_wait(LAS unsigned char* L, int flag, unsigned want) {
  POLL_BEGIN(SPIN_L)
    ok = lds_peek(L, flag) >= want;
    if (SLEEP && !ok) __builtin_amdgcn_s_sleep(1);
  POLL_END
}
DEV v4u trfrag(LAS unsigned char* p0, LAS unsigned char* p1) {
  s16x4 a = __builtin_amdgcn_ds_read_tr16_b64_v4i16((LAS s16x4*)p0), b = __builtin_amdgcn_ds_read_tr16_b64_v4i16((LAS s16x4*)p1);
  v2u x = __builtin_bit_cast(v2u, a), y = __builtin_bit_cast(v2u, b);
  return v4u{x.x, x.y, y.x, y.y};
}
template <int RS> DEV v4u wfrag(LAS unsigned char* p, int lane) {
  p += (8 * (lane >> 4) + ((lane >> 2) & 3)) * RS + (lane & 3) * 8;
  return trfrag(p, p + 4 * RS);
}
DEV f32x4 red4(LAS unsigned char* red, int lane) {
  LAS f32x4* p = (LAS f32x4*)(red + lane * 16);
  return (p[0] + p[64]) + (p[128] + p[192]);
}
DEV int h1off(int b, int n) { return WS_H1 + (n >> 6) * 2048 + ((n >> 2) & 1) * 1024 + ((n >> 3) & 7) * 128 + b * 16 + (n & 3) * 4; }
DEV int colchunk(int m, int rho) { return 8 * (m >> 1) + 2 * (((rho & 1) << 1) | (rho >> 1)) + (m & 1); }
DEV void f_dma(const Args& a, int f, int m, int lane) {
  if (m >= 14) return;
  const unsigned char* wb = (const unsigned char*)((lane & 4) ? a.w3 : a.w1) + f * 64 + (lane & 3) * 16 + (lane >> 3) * 3584;
#pragma unroll
  for (int rho = 0; rho < 4; ++rho) dma16(wb + 8 * colchunk(m, rho) * 3584, L_EW + (4 * m + rho) * 1024);
}
DEV void touch(const void* src) {
  unsigned keep;
  asm volatile("s_mov_b32 %0, m0\n\ts_mov_b32 m0, %2\n\ts_nop 0\n\tglobal_load_lds_dword %1, off nt\n\ts_mov_b32 m0, %0" : "=&s"(keep) : "v"(src), "s"(L_TCH) : "memory");
}
DEV void o_dma(const Args& a, int j, int n, int lane) {
  dma16((const unsigned char*)a.w2 + j * 32 + (lane & 1) * 16 + (32 * n + (lane >> 1)) * 896, L_EW + n * 1024);
}

DEV void shadow_wait(rsrc_t rws, unsigned e1, LAS unsigned char* L, int lane) {
  if (!(ABL & 4))
  POLL_BEGIN(SPIN_G)
    const v4u t0 = ldws(rws, WS_READY + lane * 16), t1 = ldws(rws, WS_READY + 1024 + lane * 16);
    ok = __all(((t0.x ^ e1) | (t0.y ^ ~e1) | (t0.z ^ e1) | (t0.w ^ ~e1) | (t1.x ^ e1) | (t1.y ^ ~e1) | (t1.z ^ e1) | (t1.w ^ ~e1)) == 0u);
    if (!ok) __builtin_amdgcn_s_sleep(16);
  POLL_END
  lds_signal(L, F_P, lane);
}

#define STAMP(i) do {} while (0)
#define STAMP_ACK(i) do {} while (0)
#define STAMP_OUT() do {} while (0)

__global__ void __launch_bounds__(512, 2) fused_block(Args a, Roles roles) {
  unsigned role = roles.v[blockIdx.x];
  asm volatile("" : "+s"(role), "+s"(a.kc), "+s"(a.ws));
  extern __shared__ __attribute__((aligned(16))) unsigned char smem[];
  LAS unsigned char* L = (LAS unsigned char*)smem;
  const int lane = threadIdx.x & 63, w = __builtin_amdgcn_readfirstlane(threadIdx.x >> 6);
  const int bid = blockIdx.x, xc = bid & 7, r = bid >> 3;
  const int kvh = xc, b = r & 7, s = r >> 3;
  STAMP(0);
  const rsrc_t rws = mkrsrc(a.ws, WS_BYTES);
  const bool x7 = xc == 7, isQ = !(ABL & 2) && s == 1, isM = (role >> 19) != 0u, ptype = isM | isQ;
  const int nb = role & 63, blk0 = (role >> 6) & 127, mi = (role >> 13) & 63;
  const int skip = NBMAX - nb;
  const size_t kvoff = ((size_t)(b * 8 + kvh) * 4096 + 3072 + blk0 * 8) * 256;
  const int rot = ((bid * 5 + (bid >> 3)) & 7) - skip;
  auto blkoff = [&](int n) { int t = n + rot; asm volatile("" : "+s"(t)); return (t >= nb ? t - nb : t) * 4096; };
  const int col = lane & 15, g = lane >> 4, hb = col & 7, hf = col >> 3;
  const unsigned char* qsrc = a.ws + WS_H2 + (b * 4096 + kvh * 512) * 4 + lane * 16;

  if (w < 4) {
    const int vw = w;
    __builtin_amdgcn_s_waitcnt(0x0F70);
    if (vw == 0 && lane < 32) ((LAS unsigned*)(L + L_ML))[lane] = lane < 16 ? ~0u : 0u;
    const int P1 = vw == 3 ? 3 : vw + 4;
    const int k0 = 64 * vw + 32 * hf + 8 * g, k1 = 64 * P1 + 32 * hf + 8 * g;
    auto clear_and_announce = [&]() {
      if (ABL & 4) return;
      const v4u sent = {~0u, ~0u, ~0u, ~0u};
      const int co = WS_H3 + bid * CLR_PER_WG + lane * 16;
#pragma unroll
      for (int i = 0; i < 4; ++i) stws(rws, co + i * 1024, sent);
      if (lane < 8) stws(rws, co + 4096, sent);
      const unsigned e1 = __builtin_amdgcn_readfirstlane(ldws1(rws, WS_EPOCH)) + 1u;
      waitvm<0>();
      if (lane == 0) stws2(rws, WS_READY + bid * 8, v2u{e1, ~e1});
    };
    v4u in0, in1; unsigned ep;
    const int c0 = (isQ ? bid & 63 : mi) * 64;
    if (isM || isQ) {
      const int l56 = lane < 56 ? lane : 55;
      const u16* p0 = isM ? a.c + hb * 448 + k0 : a.x + 2 * vw * 448 + l56 * 8;
      const u16* p1 = isM ? a.c + hb * 448 + k1 : a.x + (2 * vw + 1) * 448 + l56 * 8;
      asm volatile("global_load_dwordx4 %0, %3, off\n\tglobal_load_dwordx4 %1, %4, off\n\tglobal_load_dword %2, %5, off sc1"
                   : "=&v"(in0), "=&v"(in1), "=&v"(ep) : "v"(p0), "v"(p1), "v"(a.ws + WS_EPOCH) : "memory");
    }
    asm volatile("s_waitcnt lgkmcnt(0)" ::: "memory");
    __builtin_amdgcn_s_barrier();
    if (isM || isQ) {
      const int stride = isM ? 5376 : 8192;
      const unsigned char* wb = (const unsigned char*)((isM ? a.wmod : a.wq) + c0) + (lane >> 3) * stride + (lane & 7) * 16;
#pragma unroll
      for (int i = 0; i < 8; ++i) dma16(wb + (8 * vw + i) * 8 * stride, L_EW + (8 * vw + i) * 1024);
      waitvm<8>();
      asm volatile("" : "+v"(in0), "+v"(in1), "+v"(ep)::"memory");
      if (vw < 3) {
#pragma unroll
        for (int i = 0; i < 8; ++i) dma16(wb + (8 * vw + 32 + i) * 8 * stride, L_EW + (8 * vw + 32 + i) * 1024);
      }
      lds_signal(L, F_GO, lane);
      STAMP(1);
      const unsigned tag = (__builtin_amdgcn_readfirstlane(ep) + 1u) & 0xffffu;
      v4u act[2];
      if (isM) {
#pragma unroll
        for (int i = 0; i < 4; ++i) { act[0][i] = pk(silu_p(lo16(in0[i])), silu_p(hi16(in0[i]))); act[1][i] = pk(silu_p(lo16(in1[i])), silu_p(hi16(in1[i]))); }
      }
      if (isQ) {
#pragma unroll
        for (int rr = 0; rr < 2; ++rr) {
          const v4u in = rr ? in1 : in0;
          float t[8];
#pragma unroll
          for (int i = 0; i < 4; ++i) { t[2 * i] = lo16(in[i]); t[2 * i + 1] = hi16(in[i]); }
          float sm = 0.f;
#pragma unroll
          for (int i = 0; i < 8; ++i) sm += t[i];
          const float mean = wavesum(lane < 56 ? sm : 0.f) * (1.f / 448.f);
          float sq = 0.f;
#pragma unroll
          for (int i = 0; i < 8; ++i) { const float d = t[i] - mean; sq += d * d; }
          const float var = rb(wavesum(lane < 56 ? sq : 0.f) * (1.f / 448.f)), mb = rb(mean);
          const float sd = rb(sqrtf(rb(var + 1e-5f)));
          v4u o;
#pragma unroll
          for (int i = 0; i < 4; ++i) o[i] = pk(rb(t[2 * i] - mb) / sd, rb(t[2 * i + 1] - mb) / sd);
          if (lane < 56) *(LAS v4u*)(L + L_XN + ((2 * vw + rr) * 448 + lane * 8) * 2) = o;
        }
        lds_signal(L, F_XN, lane);
      }
      SB();
      v4u wf[2][2][4];
#pragma unroll
      for (int pi = 0; pi < 2; ++pi)
#pragma unroll
        for (int h2 = 0; h2 < 2; ++h2) {
          if (pi == 0 && h2 == 0) { if (vw < 3) waitvm<8>(); else waitvm<0>(); }
          if (pi == 1 && h2 == 0) waitvm<4>();
          if (pi == 1 && h2 == 1) { waitvm<0>(); STAMP(2); }
#pragma unroll
          for (int ct = 0; ct < 4; ++ct) wf[pi][h2][ct] = wfrag<128>(L + L_EW + (64 * (pi ? P1 : vw) + 32 * h2) * 128 + ct * 32, lane);
        }
      if (isQ) {
        if (vw == 3) clear_and_announce();
        lds_wait(L, F_XN, 4);
        const v4u xn0 = *(LAS v4u*)(L + L_XN + (hb * 448 + k0) * 2), xn1 = *(LAS v4u*)(L + L_XN + (hb * 448 + k1) * 2);
        POLL_BEGIN(SPIN_G)
          const unsigned t = ldws1(rws, WS_H1 + (((lane & 1) ? P1 : vw) + 7 * ((lane >> 1) & 1)) * 2048 + 512);
          ok = __all(((t ^ tag) & 0xffffu) == 0u);
          if (!ok) __builtin_amdgcn_s_sleep(1);
        POLL_END
        v4u m[8];
        POLL_BEGIN(SPIN_G)
          const int o0 = WS_H1 + vw * 2048 + hf * 512 + (g * 8 + hb) * 16, o1 = o0 + (P1 - vw) * 2048;
          m[0] = ldws(rws, o0); m[1] = ldws(rws, o0 + 1024); m[2] = ldws(rws, o0 + 14336); m[3] = ldws(rws, o0 + 15360);
          m[4] = ldws(rws, o1); m[5] = ldws(rws, o1 + 1024); m[6] = ldws(rws, o1 + 14336); m[7] = ldws(rws, o1 + 15360);
          SB();
          if (sp == SPIN_G) lds_signal(L, F_GO, lane);
          unsigned t = 0;
#pragma unroll
          for (int i = 0; i < 8; ++i) t = tagx(t, m[i], tag);
          ok = __all((t & 0xffffu) == 0u);
        POLL_END
#pragma unroll
        for (int pi = 0; pi < 2; ++pi) {
          const v4u xn = pi ? xn1 : xn0;
#pragma unroll
          for (int i = 0; i < 4; ++i) {
            const v4u sa = m[4 * pi + (i >> 1)], ca = m[4 * pi + 2 + (i >> 1)];
            const int j = 2 * (i & 1);
            const unsigned c2 = pk(1.f + hi16(ca[j]), 1.f + hi16(ca[j + 1]));
            const unsigned t2 = pk(lo16(xn[i]) * lo16(c2), hi16(xn[i]) * hi16(c2));
            act[pi][i] = pk(lo16(t2) + hi16(sa[j]), hi16(t2) + hi16(sa[j + 1]));
          }
        }
      }
      if (vw == 3) act[1] = v4u{0u, 0u, 0u, 0u};
      STAMP(3);
#pragma unroll
      for (int t2 = 0; t2 < 2; ++t2) {
        const int ct = 2 * t2;
        f32x4 a0 = mfma(wf[0][0][ct], act[0], ZERO4), a1 = mfma(wf[0][0][ct + 1], act[0], ZERO4);
        f32x4 b0 = mfma(wf[0][1][ct], act[0], ZERO4), b1 = mfma(wf[0][1][ct + 1], act[0], ZERO4);
        a0 = mfma(wf[1][0][ct], act[1], a0); a1 = mfma(wf[1][0][ct + 1], act[1], a1);
        b0 = mfma(wf[1][1][ct], act[1], b0); b1 = mfma(wf[1][1][ct + 1], act[1], b1);
        f32x4 o;
#pragma unroll
        for (int i = 0; i < 4; ++i) o[i] = (hf ? b1[i] : a0[i]) + dpp<ROR8>(hf ? b0[i] : a1[i]);
        *(LAS f32x4*)(L + L_RED + t2 * 4096 + vw * 1024 + lane * 16) = o;
      }
      lds_signal(L, F_RED, lane);
      if (vw < 2) {
        lds_wait<false>(L, F_RED, 4);
        const f32x4 o = red4(L + L_RED + vw * 4096, lane);
        const unsigned p01 = pk(o[0], o[1]), p23 = pk(o[2], o[3]);
        const int n = c0 + 32 * vw + 16 * hf + 4 * g;
        stws(rws, isM ? h1off(hb, n) : WS_H2 + (hb * 4096 + n) * 4,
             v4u{(p01 << 16) | tag, (p01 & 0xffff0000u) | tag, (p23 << 16) | tag, (p23 & 0xffff0000u) | tag});
      }
    }
    STAMP(10);
    if (vw == 3 && !isQ) clear_and_announce();
    v4u v[NBMAX], f[NBMAX / 8][8];
#pragma unroll
    for (int n = 0; n < NBMAX - NBMIN; ++n) { v[n] = v4u{0u, 0u, 0u, 0u}; asm volatile("" : "+v"(v[n])); }
    STAMP(4);
    __builtin_amdgcn_s_barrier();
    STAMP(5);
    const u64 vbase = (u64)(a.vc + kvoff);
    const v4u rv = {(unsigned)vbase, (unsigned)(vbase >> 32) & 0xffffu, (unsigned)nb * 4096u, 0x00020000u};
    const int vvo = (lane >> 3) * 512 + vw * 128 + (lane & 7) * 16;
    auto xpose = [&](int n) {
      asm volatile("" : "+v"(v[n]), "+v"(v[n + 1]));
#pragma unroll
      for (int j = 0; j < 8; ++j) f[n >> 3][j][(n & 7) >> 1] = (j & 1) ? hi2(v[n][j >> 1], v[n + 1][j >> 1]) : lo2(v[n][j >> 1], v[n + 1][j >> 1]);
    };
    static_assert(DEPTH_B <= 9, "tail of the V stream");
    f32x4 acc[8];
#pragma unroll
    for (int j = 0; j < 8; ++j) acc[j] = ZERO4;
#pragma unroll
    for (int n = 0; n < NBMAX; ++n) {
      if (n >= NBMAX - NBMIN) {
        asm volatile("buffer_load_dwordx4 %0, %1, %2, %3 offen nt" : "=v"(v[n]) : "v"(vvo), "s"(rv), "s"(blkoff(n)) : "memory");
        if (vw == 0 && ((n + 1 == NBMAX - LATE_W && r >= 18) || (n + 1 == NBMAX - LATE_F && r < 18))) lds_signal(L, F_LATE, lane);
        if (n + 1 < NBMAX) waitvm<DEPTH_B - 1>();
      } else if (n >= skip) {
        asm volatile("buffer_load_dwordx4 %0, %1, %2, %3 offen nt" : "+v"(v[n]) : "v"(vvo), "s"(rv), "s"(blkoff(n)) : "memory");
        waitvm<DEPTH_B - 1>();
      }
      if (n == 33 || n == 35 || n == 37) {
        const int G = (n - 33) >> 1;
#pragma unroll
        for (int q = 0; q < 4; ++q) xpose(8 * G + 2 * q);
#pragma unroll
        for (int j = 0; j < 8; ++j) asm volatile("" : "+v"(f[G][j]));
      }
    }
    STAMP(6);
    __builtin_amdgcn_s_setprio(3);
    waitvm<8>();
#pragma unroll
    for (int q = 0; q < 4; ++q) xpose(24 + 2 * q);
#pragma unroll
    for (int j = 0; j < 8; ++j) asm volatile("" : "+v"(f[3][j]));
    lds_wait<false>(L, F_P, 5);
    STAMP(7);
    v4u pf[5];
#pragma unroll
    for (int G = 0; G < 5; ++G) pf[G] = *(LAS v4u*)(L + L_P + ((G * 2 + ((col >> 2) & 1)) * 8 + 2 * g + (col & 1)) * 16);
    SB();
#pragma unroll
    for (int G = 0; G < 4; ++G)
#pragma unroll
      for (int j = 0; j < 8; ++j) acc[j] = mfma(pf[G], f[G][j], acc[j]);
    const int pubo = WS_H3 + (((2 * kvh + g) * 4 + vw) * 2 + (b >> 2)) * 2048 + (4 * (b & 3) + s) * 128 + hb * 16;
    asm volatile("" ::"v"(pubo));
    SB();
    waitvm<6>(); xpose(32); SB();
    waitvm<4>(); xpose(34); SB();
    waitvm<2>(); xpose(36); SB();
    waitvm<0>(); xpose(38);
#pragma unroll
    for (int j = 0; j < 8; ++j) acc[j] = mfma(pf[4], f[4][j], acc[j]);
    float o[8];
#pragma unroll
    for (int j = 0; j < 8; ++j) o[j] = acc[j][0] + dpp<ROR8>(acc[j][1]);
    const v4u ov = nosent(v4u{pk(o[0], o[1]), pk(o[2], o[3]), pk(o[4], o[5]), pk(o[6], o[7])});
    if ((lane & 0x28) == 0) stws(rws, pubo, ov);
    lds_signal(L, F_PUB, lane);
    STAMP(8);
    STAMP_ACK(9);
    STAMP_OUT();
    EXIT();
  }

  const int kw = w - 4;
  unsigned tag, ep = 0;
  {
    const rsrc_t rk = mkrsrc(a.kc + kvoff, nb * 4096);
    const int vo = ((lane >> 5) * 4 + ((lane >> 2) & 3)) * 512 + (((lane >> 4) & 1) * 4 + (lane & 3)) * 16;
    v4u k[NBMAX];
    const unsigned tstart = (unsigned)__builtin_amdgcn_s_memrealtime();
    if (ptype) { __builtin_amdgcn_s_barrier(); lds_wait(L, F_GO, 4); }
    STAMP(15);
    int issued = 0;
    unsigned qnext = tstart + QT0;
#pragma unroll
    for (int i = 0; i < NBMAX / 4; ++i) {
      if (4 * i >= NBMAX - NBMIN || 4 * i + kw >= skip) {
        const int so = blkoff(4 * i + kw);
#pragma unroll
        for (int j = 0; j < 4; ++j) {
          k[4 * i + j] = __builtin_amdgcn_raw_buffer_load_b128(rk, vo + j * 128, so, AUX_NT);
          SB();
          if (4 * i + j + 1 < NBMAX) { if (i < NSW) waitvm<DEPTH_A - 1>(); else waitvm<DEPTH_B - 1>(); }
        }
        if (isQ && ++issued == KQ1) lds_wait(L, F_GO, 8);
      }
      if (i == NBMAX / 4 - 3) { ep = ldws1(rws, WS_EPOCH); SB(); }
      if (i == 1 && !ptype) __builtin_amdgcn_s_barrier();
    }
    STAMP(1);
    tag = (__builtin_amdgcn_readfirstlane(ep) + 1u) & 0xffffu;
    if (kw < 2) *(LAS v4u*)(L + L_Q + kw * 1024 + lane * 16) = v4u{~tag, ~tag, ~tag, ~tag};
    asm volatile("s_waitcnt lgkmcnt(0)" ::: "memory");
    __builtin_amdgcn_s_barrier();
    STAMP(2);
    __builtin_amdgcn_s_setprio(2);
    if (kw == 1) { shadow_wait(rws, ep + 1u, L, lane); STAMP(6); }
    __builtin_amdgcn_s_waitcnt(0x0F70);
    v4u qf[4];
    {
      v4u qt[8];
      POLL_BEGIN(SPIN_L)
        unsigned t = 0;
#pragma unroll
        for (int j = 0; j < 8; ++j) { qt[j] = *(volatile LAS v4u*)(L + L_Q + (((col >> 2) & 1) * 256 + 64 * (j >> 1) + 8 * (4 * (g & 1) + (col & 3))) * 4 + (j & 1) * 16); t = tagx(t, qt[j], tag); }
        ok = (ABL & 1) || __all((t & 0xffffu) == 0u);
        if (!ok) {
          if (kw == 0) {
            const unsigned now = (unsigned)__builtin_amdgcn_s_memrealtime();
            if ((int)(now - qnext) >= 0) { dma16_sc1(qsrc, L_Q); dma16_sc1(qsrc + 1024, L_Q + 1024); qnext = now + QDT; }
          }
          __builtin_amdgcn_s_sleep(1);
        }
      POLL_END
      STAMP(3);
      const unsigned keep = (g >> 1) == hf ? ~0u : 0u;
#pragma unroll
      for (int j = 0; j < 4; ++j) qf[j] = v4u{hi2(qt[2 * j].x, qt[2 * j].y) & keep, hi2(qt[2 * j].z, qt[2 * j].w) & keep, hi2(qt[2 * j + 1].x, qt[2 * j + 1].y) & keep, hi2(qt[2 * j + 1].z, qt[2 * j + 1].w) & keep};
    }
    float S[3];
    const int bb = lane & 3;
#pragma unroll
    for (int i = 0; i < 10; ++i) {
      f32x4 d = ZERO4;
#pragma unroll
      for (int j = 0; j < 4; ++j) d = mfma(qf[j], k[4 * i + j], d);
      const float sc = (dpp<0x00>(d[0]) + dpp<0x55>(d[1])) + (dpp<0xAA>(d[2]) + dpp<0xFF>(d[3]));
      if ((i & 3) == 0) S[i >> 2] = sc; else S[i >> 2] = bb == (i & 3) ? sc : S[i >> 2];
    }
    STAMP(4);
#pragma unroll
    for (int u = 0; u < 3; ++u) S[u] = (unsigned)(16 * u + 4 * bb + kw - skip) < (unsigned)nb ? rb(S[u]) * 0.0625f : -INFINITY;
    const int hh = g & 1;
    const float mw = max32(rowmax(fmaxf(fmaxf(S[0], S[1]), S[2])));
    if ((lane & 0x2f) == 0) *(volatile LAS unsigned*)(L + L_ML + (hh * 4 + kw) * 4) = nosent(asu(mw));
    f32x4 m4;
    POLL_BEGIN(SPIN_L)
      m4 = *(volatile LAS f32x4*)(L + L_ML + hh * 16);
      ok = __all(smax(0u, __builtin_bit_cast(v4u, m4)) != ~0u);
    POLL_END
    const float m = fmaxf(fmaxf(m4[0], m4[1]), fmaxf(m4[2], m4[3]));
    const float e0 = ex2((S[0] - m) * LOG2E), e1 = ex2((S[1] - m) * LOG2E), e2 = ex2((S[2] - m) * LOG2E);
    {
      const unsigned pp = pk(e0, e1), p2 = pk(e2, e2);
      LAS unsigned char* pb = L + L_P + (((bb >> 1) * 2 + hh) * 8 + 4 * (g >> 1) + (col >> 2)) * 16 + (4 * (bb & 1) + kw) * 2;
      *(LAS u16*)pb = (u16)pp;
      *(LAS u16*)(pb + 512) = (u16)(pp >> 16);
      if (bb < 2) *(LAS u16*)(pb + 1024) = (u16)p2;
    }
    lds_signal(L, F_P, lane);
    STAMP(5);
    float lw = rowsum((e0 + e1) + e2);
    lw = sum32swap(lw, lw);
    if ((lane & 0x2f) == 0) *(LAS float*)(L + L_ML + 32 + (hh * 4 + kw) * 4) = lw;
    lds_signal(L, F_L, lane);
    if (kw == 0) {
      lds_wait(L, F_L, 4);
      const f32x4 l4 = *(LAS f32x4*)(L + L_ML + 32 + hh * 16);
      if ((lane & 0x2f) == 0) stws2(rws, WS_STATS + (b * 8 + kvh) * 64 + s * 16 + hh * 8, v2u{asu((l4[0] + l4[1]) + (l4[2] + l4[3])), (asu(m) & 0xffff0000u) | tag});
    }
    __builtin_amdgcn_s_setprio(0);
  }

  if (ABL & 8) { STAMP_OUT(); EXIT(); }
  if (r >= 18) {
    const int h = 2 * ((xc + 4) & 7) + (r >= 25), cb64 = r >= 25 ? r - 25 : r - 18, khalf = kw >> 1, rt = kw & 1;
    const int tb = xc, m = r - 18;
    const int rcol = colchunk(m, g) * 8 + hb;
    const float xv = lo16(a.x[tb * 448 + rcol]);
    lds_wait(L, F_LATE, 1);
    {
      const unsigned char* wb = (const unsigned char*)(a.wo + (size_t)h * 256 * 448 + cb64 * 64) + (lane >> 3) * 896 + (lane & 7) * 16;
#pragma unroll
      for (int i = 0; i < 8; ++i) dma16(wb + (8 * kw + i) * 8 * 896, L_EW + (8 * kw + i) * 1024);
    }
    float wgt[4], gaf, cc;
    {
      unsigned ga, cb;
      v2u pe[4];
      POLL_BEGIN(SPIN_G)
        ga = ldws1(rws, h1off(tb, 2 * 448 + rcol)); cb = ldws1(rws, h1off(tb, 4 * 448 + rcol));
        unsigned t2 = (ga ^ tag) | (cb ^ tag);
#pragma unroll
        for (int i = 0; i < 4; ++i) { pe[i] = ldws2(rws, WS_STATS + ((4 * rt + g) * 8 + (h >> 1)) * 64 + i * 16 + (h & 1) * 8); t2 |= pe[i].y ^ tag; }
        ok = __all((t2 & 0xffffu) == 0u);
        if (!ok) __builtin_amdgcn_s_sleep(4);
      POLL_END
      const float M = fmaxf(fmaxf(hi16(pe[0].y), hi16(pe[1].y)), fmaxf(hi16(pe[2].y), hi16(pe[3].y)));
#pragma unroll
      for (int i = 0; i < 4; ++i) wgt[i] = ex2((hi16(pe[i].y) - M) * LOG2E);
      const float iL = 1.f / ((wgt[0] * asf(pe[0].x) + wgt[1] * asf(pe[1].x)) + (wgt[2] * asf(pe[2].x) + wgt[3] * asf(pe[3].x)));
#pragma unroll
      for (int i = 0; i < 4; ++i) wgt[i] *= iL;
      gaf = hi16(ga); cc = rb(1.f + hi16(cb));
    }
    waitvm<0>();
    STAMP(7);
    lds_signal(L, F_WLD, lane);
    lds_wait<false>(L, F_PUB, 4);
    const int o0 = WS_H3 + ((h * 4 + 2 * khalf) * 2 + rt) * 2048 + hb * 128 + hf * 64 + g * 16;
    const int yo = WS_H4 + (((4 * rt + g) * 14 + 2 * cb64 + hf) * 32 + 2 * h + khalf) * 128 + hb * 16;
    v4u of[4];
    of[0] = ldws(rws, o0); of[1] = ldws(rws, o0 + 1024); of[2] = ldws(rws, o0 + 4096); of[3] = ldws(rws, o0 + 5120);
    SB();
    lds_wait<false>(L, F_WLD, 4);
    v4u wf[4][4];
#pragma unroll
    for (int i = 0; i < 4; ++i)
#pragma unroll
      for (int ct = 0; ct < 4; ++ct) { wf[i][ct] = wfrag<128>(L + L_EW + (128 * khalf + 32 * i) * 128 + ct * 32, lane); asm volatile("" : "+v"(wf[i][ct])); }
    asm volatile("" ::"v"(yo));
    STAMP(8);
    if (!__all(smax(smax(smax(smax(0u, of[0]), of[1]), of[2]), of[3]) != ~0u)) {
      POLL_BEGIN(SPIN_G)
        of[0] = ldws(rws, o0); of[1] = ldws(rws, o0 + 1024); of[2] = ldws(rws, o0 + 4096); of[3] = ldws(rws, o0 + 5120);
        ok = __all(smax(smax(smax(smax(0u, of[0]), of[1]), of[2]), of[3]) != ~0u);
      POLL_END
    }
    STAMP(9);
    {
      f32x4 d[4] = {ZERO4, ZERO4, ZERO4, ZERO4};
#pragma unroll
      for (int i = 0; i < 4; ++i) {
        v4u fr;
#pragma unroll
        for (int e = 0; e < 4; ++e) fr[e] = (i & 1) ? half_from<0x3>(of[i][e], of[i - 1][e]) : half_from<0xC>(of[i][e], of[i + 1][e]);
#pragma unroll
        for (int ct = 0; ct < 4; ++ct) d[ct] = mfma(fr, wf[i][ct], d[ct]);
      }
      v4u o;
#pragma unroll
      for (int ct = 0; ct < 4; ++ct) o[ct] = nosent(asu(fmaf(d[ct][0], wgt[0], d[ct][1] * wgt[1]) + fmaf(d[ct][2], wgt[2], d[ct][3] * wgt[3])));
      stws(rws, yo, o);
    }
    STAMP(10);
    if (kw != 0) { STAMP_ACK(11); STAMP_OUT(); EXIT(); }
    v4u y[4];
    POLL_BEGIN(SPIN_G)
#pragma unroll
      for (int i = 0; i < 4; ++i) y[i] = ldws(rws, WS_H4 + (tb * 14 + m) * 4096 + i * 1024 + lane * 16);
      ok = __all(smax(smax(smax(smax(0u, y[0]), y[1]), y[2]), y[3]) != ~0u);
    POLL_END
    STAMP(12);
    const f32x4 sv = (asf4(y[0]) + asf4(y[1])) + (asf4(y[2]) + asf4(y[3]));
    float u = sum16swap(sum32swap(sv[0], sv[1]), sum32swap(sv[2], sv[3]));
    u += dpp<ROR8>(u);
    const float x1 = rb(xv + rb(gaf * rb(u))), val = hf ? x1 : x1 * cc;
    const unsigned d1 = nosent(pk(dpp<0xA0>(val), dpp<0xF5>(val)));
    const unsigned e0 = dppu<0x00>(d1), e1 = dppu<0xAA>(d1), f0 = dppu<HMIRROR>(e0), f1 = dppu<HMIRROR>(e1);
    const bool q1 = (lane & 4) != 0;
    stws(rws, WS_H5 + hb * 14336 + (m * 8 + tb) * 128 + hf * 64 + g * 16, v4u{q1 ? f0 : e0, q1 ? f1 : e1, q1 ? e0 : f0, q1 ? e1 : f1});
    STAMP(13);
    STAMP_ACK(14);
    STAMP_OUT();
    EXIT();
  }

  if (x7) { STAMP_OUT(); EXIT(); }
  lds_wait(L, F_LATE, 1);
#pragma unroll
  for (int i = 0; i < 2; ++i) {
    const int t = (i * 72 + r * 4 + kw) * 64 + lane;
    if (i * 72 + r * 4 + kw < 84)
      touch(t < 3584 ? (const unsigned char*)(t < 1792 ? a.w1 : a.w3 - 448 * 1792) + (t >> 2) * 3584 + xc * 512 + (t & 3) * 128 : (const unsigned char*)a.w2 + (t - 3584) * 896 + xc * 128);
  }
  if (!isQ && (s != 0 || b < 4)) { STAMP_OUT(); EXIT(); }
  lds_wait(L, F_PUB, 4);

  if (isQ) {
    const int f = 8 * xc + b;
    v4u cs[4];
    {
      v4u m[8];
      POLL_BEGIN(SPIN_G)
        unsigned t = 0;
#pragma unroll
        for (int i = 0; i < 4; ++i) {
          const int o = h1off(hb, (hf ? 3 : 4) * 448 + 8 * colchunk(min(kw + 4 * i, 13), g));
          m[2 * i] = ldws(rws, o); m[2 * i + 1] = ldws(rws, o + 1024);
        }
#pragma unroll
        for (int i = 0; i < 8; ++i) t = tagx(t, m[i], tag);
        ok = __all((t & 0xffffu) == 0u);
        if (!ok) __builtin_amdgcn_s_sleep(4);
      POLL_END
      const float one = hf ? 0.f : 1.f;
#pragma unroll
      for (int i = 0; i < 4; ++i)
#pragma unroll
        for (int e = 0; e < 4; ++e) cs[i][e] = pk(one + hi16(m[2 * i + (e >> 1)][2 * (e & 1)]), one + hi16(m[2 * i + (e >> 1)][2 * (e & 1) + 1]));
    }
#pragma unroll
    for (int i = 0; i < 4; ++i) f_dma(a, f, kw + 4 * i, lane);
    v4u wf[4][4];
#pragma unroll
    for (int i = 0; i < 4; ++i) {
      if (i == 0) waitvm<8>();
      if (i == 1) waitvm<4>();
      if (i == 2) { waitvm<0>(); STAMP(7); }
#pragma unroll
      for (int ct = 0; ct < 4; ++ct) {
        wf[i][ct] = wfrag<128>(L + L_EW + (i == 3 && kw >= 2 ? kw : kw + 4 * i) * 4096 + ct * 32, lane);
        if (i == 3 && kw >= 2) wf[i][ct] = v4u{0u, 0u, 0u, 0u};
      }
    }
#pragma unroll
    for (int ct = 0; ct < 4; ++ct) {
      f32x4 t = ZERO4;
#pragma unroll
      for (int i = 0; i < 4; ++i) t = mfma(wf[i][ct], cs[i], t);
      *(LAS f32x4*)(L + L_RED + ct * 4096 + kw * 1024 + lane * 16) = t;
    }
    asm volatile("s_waitcnt lgkmcnt(0)" ::: "memory");
    __builtin_amdgcn_s_barrier();
    const f32x4 T1 = red4(L + L_RED + (2 * hf + (kw & 1)) * 4096, lane & ~8), T2 = red4(L + L_RED + (2 * hf + (kw & 1)) * 4096, lane | 8);
    asm volatile("s_waitcnt lgkmcnt(0)" ::: "memory");
    __builtin_amdgcn_s_barrier();
    STAMP(8);
    v4u zz[4];
    POLL_BEGIN(SPIN_G)
#pragma unroll
      for (int i = 0; i < 4; ++i) zz[i] = ldws(rws, WS_H5 + xc * 14336 + min(kw + 4 * i, 13) * 1024 + hb * 128 + hf * 64 + g * 16);
      ok = __all(smax(smax(smax(smax(0u, zz[0]), zz[1]), zz[2]), zz[3]) != ~0u);
    POLL_END
    STAMP(9);
    {
      f32x4 s1 = ZERO4, s2 = ZERO4;
#pragma unroll
      for (int tp = 0; tp < 2; ++tp) {
        f32x4 ag = ZERO4, au = ZERO4;
#pragma unroll
        for (int i = 0; i < 4; ++i) { ag = mfma(wf[i][tp], zz[i], ag); au = mfma(wf[i][2 + tp], zz[i], au); }
        v4u o;
#pragma unroll
        for (int i = 0; i < 4; ++i) o[i] = half_from<0xC>(asu(ag[i]), asu(au[i]));
        *(LAS v4u*)(L + L_RED + tp * 4096 + kw * 1024 + lane * 16) = o;
      }
      const v4u ones = {0x3f803f80u, 0x3f803f80u, 0x3f803f80u, 0x3f803f80u};
#pragma unroll
      for (int i = 0; i < 3; ++i) { s1 = mfma(ones, zz[i], s1); s2 = mfma(zz[i], zz[i], s2); }
      if (kw < 2) { s1 = mfma(ones, zz[3], s1); s2 = mfma(zz[3], zz[3], s2); }
      const int cr = col & 3;
      const float dg = cr == 0 ? s2[0] : cr == 1 ? s2[1] : cr == 2 ? s2[2] : s2[3];
      if (hf && (col >> 2) == g) *(LAS f32x2*)(L + L_STK + (hb * 4 + kw) * 8) = f32x2{s1[0], dg};
    }
    asm volatile("s_waitcnt lgkmcnt(0)" ::: "memory");
    STAMP(10);
    __builtin_amdgcn_s_barrier();
    STAMP(11);
    const f32x4 Z = red4(L + L_RED + (kw & 1) * 4096, lane);
    LAS f32x4* sp4 = (LAS f32x4*)(L + L_STK + hb * 32);
    const f32x4 t0 = sp4[0] + sp4[1];
    const float mu = (t0[0] + t0[2]) * (1.f / 448.f), var = fmaxf((t0[1] + t0[3]) * (1.f / 448.f) - mu * mu, 0.f);
    const float rs = __builtin_amdgcn_rsqf(var + 1e-5f);
    float val[4];
#pragma unroll
    for (int i = 0; i < 4; ++i) val[i] = rb(fmaf(rs, fmaf(-mu, T1[i], Z[i]), T2[i]));
    float hv[2];
#pragma unroll
    for (int i = 0; i < 2; ++i) {
      const float gt = asf(half_from<0xC>(asu(val[i]), asu(val[2 + i]))), up = asf(half_from<0x3>(asu(val[2 + i]), asu(val[i])));
      hv[i] = rb(silu(gt)) * up;
    }
    const unsigned hp = nosent(pk(hv[0], hv[1])), ho = dppu<ROR8>(hp);
    STAMP(12);
#pragma unroll
    for (int i = 0; i < 2; ++i) stws2(rws, WS_H6 + (4 * (kw >> 1) + 2 * i + hf) * 28672 + (2 * f + (kw & 1)) * 256 + hb * 32 + g * 8, v2u{hf ? ho : hp, hf ? hp : ho});
    STAMP(13);
    STAMP_ACK(14);
    STAMP_OUT();
    EXIT();
  }

  {
    const int j = 4 * xc + b - 4;
#pragma unroll
    for (int i = 0; i < 14; ++i) o_dma(a, j, 14 * kw + i, lane);
    const int oc = 16 * j + 4 * g;
    v4u gb;
    POLL_BEGIN(SPIN_G)
      gb = ldws(rws, h1off(hb, 5 * 448 + oc));
      ok = __all((tagx(0u, gb, tag) & 0xffffu) == 0u);
      if (!ok) __builtin_amdgcn_s_sleep(4);
    POLL_END
    waitvm<0>();
    STAMP(7);
    v4u wf[7][2];
#pragma unroll
    for (int i = 0; i < 7; ++i)
#pragma unroll
      for (int h2 = 0; h2 < 2; ++h2) wf[i][h2] = wfrag<32>(L + L_EW + (14 * kw + 2 * i + h2) * 1024, lane);
    v2u xz;
    {
      const int c8 = oc >> 3, mm = 2 * (c8 >> 3) + (c8 & 1), ct2 = (c8 >> 1) & 3, rh = ((ct2 & 1) << 1) | (ct2 >> 1);
      POLL_BEGIN(SPIN_G)
        xz = ldws2(rws, WS_H5 + xc * 14336 + (mm * 8 + hb) * 128 + 64 + rh * 16 + (oc & 4) * 2);
        ok = __all(max(xz.x, xz.y) != ~0u);
        if (!ok) __builtin_amdgcn_s_sleep(2);
      POLL_END
    }
    STAMP(8);
    v4u hh[7];
    POLL_BEGIN(SPIN_G)
      unsigned mx = 0;
#pragma unroll
      for (int i = 0; i < 7; ++i) hh[i] = ldws(rws, WS_H6 + xc * 28672 + (7 * kw + i) * 1024 + (2 * hf + (g >> 1)) * 256 + hb * 32 + (g & 1) * 16);
#pragma unroll
      for (int i = 0; i < 7; ++i) mx = smax(mx, hh[i]);
      ok = __all(mx != ~0u);
    POLL_END
    STAMP(9);
    {
      f32x4 a0 = ZERO4, a1 = ZERO4;
#pragma unroll
      for (int i = 0; i < 7; ++i) { a0 = mfma(wf[i][0], hh[i], a0); a1 = mfma(wf[i][1], hh[i], a1); }
      f32x4 o;
#pragma unroll
      for (int i = 0; i < 4; ++i) o[i] = a0[i] + dpp<ROR8>(a1[i]);
      *(LAS f32x4*)(L + L_RED + kw * 1024 + lane * 16) = o;
    }
    asm volatile("s_waitcnt lgkmcnt(0)" ::: "memory");
    STAMP(10);
    __builtin_amdgcn_s_barrier();
    if (kw == 0) {
      const f32x4 Y = red4(L + L_RED, lane);
      const unsigned y01 = pk(Y[0], Y[1]), y23 = pk(Y[2], Y[3]);
      const unsigned t01 = pk(hi16(gb.x) * lo16(y01), hi16(gb.y) * hi16(y01)), t23 = pk(hi16(gb.z) * lo16(y23), hi16(gb.w) * hi16(y23));
      const v2u o = {pk(lo16(xz.x) + lo16(t01), hi16(xz.x) + hi16(t01)), pk(lo16(xz.y) + lo16(t23), hi16(xz.y) + hi16(t23))};
      if (hf == 0) *(v2u*)(a.out + hb * 448 + oc) = o;
      if (j == 0 && lane == 0) __builtin_amdgcn_raw_buffer_store_b32(ep + 1u, rws, WS_EPOCH, 0, AUX_SC1);
      STAMP(11);
      STAMP_ACK(12);
    }
  }
  STAMP_OUT();
}

extern "C" void kernel_launch(void* const* d_in, const int* in_sizes, int n_in, void* d_out, int out_size, void* d_ws, size_t ws_size, hipStream_t stream) {
  Args a;
  a.x = (const u16*)d_in[0]; a.kc = (const u16*)d_in[1]; a.vc = (const u16*)d_in[2]; a.wq = (const u16*)d_in[3]; a.wo = (const u16*)d_in[4];
  a.w1 = (const u16*)d_in[6]; a.w3 = (const u16*)d_in[7]; a.w2 = (const u16*)d_in[8]; a.c = (const u16*)d_in[9]; a.wmod = (const u16*)d_in[10];
  a.out = (u16*)d_out; a.ws = (unsigned char*)d_ws + (WS_AT_END ? ((ws_size - WS_BYTES) & ~(size_t)0x1fffff) : 0);
  Roles roles;
  for (int qd = 0; qd < 64; ++qd) {
    const int xc = qd & 7, b = qd >> 3;
    int mi[4] = {-1, -1, -1, -1};
    if (xc < 7 && b < 4) mi[0] = 7 * b + xc;
    if (xc < 7 && (b == 4 || b == 5) && DUP_M) mi[2] = 7 * (b - 4) + (xc + 3) % 7;
    if (xc == 7 && b < 7) { mi[0] = 28 + b; mi[2] = 35 + b; }
    int n[4];
    n[1] = NB_Q;
    const int rest = 128 - n[1] + CUT_M * ((mi[0] >= 0) + (mi[2] >= 0));
    n[0] = (rest + 2) / 3 - (mi[0] >= 0 ? CUT_M : 0); n[2] = (rest + 1) / 3 - (mi[2] >= 0 ? CUT_M : 0); n[3] = rest / 3;
    for (int s = 0, first = 0; s < 4; first += n[s], ++s)
      roles.v[s * 64 + qd] = (ABL & 2) ? (32u | (32u * s) << 6) : (unsigned)(n[s] | first << 6 | (mi[s] >= 0 ? (mi[s] << 13 | 1 << 19) : 0));
  }
  (void)hipFuncSetAttribute((const void*)fused_block, hipFuncAttributeMaxDynamicSharedMemorySize, L_BYTES);
  fused_block<<<256, 512, L_BYTES, stream>>>(a, roles);
}
```

```cpp
#pragma clang diagnostic ignored "-Wint-to-pointer-cast"
#include <hip/hip_runtime.h>
#include <hip/hip_bf16.h>
#include <stdint.h>
#include <type_traits>

constexpr int CUT_Q = 11, CUT_C = 12, CUT_M = 10, CUT_W = 4, CUT_O = 0;
constexpr int DEPTH_A = 4, PACE_K = 3;
constexpr int DEPTH_B = 6, QDEPTH = 8, EDEPTH = 6;
constexpr int TEND = 1150, TLAT = 94, TLATK = 90;
constexpr int TOFF = 130, ALAG = 35, VLAG = 25, TREF_AT = 5;
constexpr int GT0 = 400, QT0 = 720, QDT = 120;
constexpr int RB_DELAY = 8, O_DELAY = 16, TSPLIT = 12;
constexpr int CODE_CH = 40;
constexpr int NBMAX = 40, NBMIN = 20;
static_assert((131 + CUT_C + CUT_Q + CUT_W) / 4 <= NBMAX && (131 + CUT_M + CUT_Q + CUT_W) / 4 <= NBMAX && (131 + CUT_M + CUT_Q + 2 * CUT_W) / 4 - CUT_W <= NBMAX && (131 + CUT_Q + 2 * CUT_W) / 4 <= NBMAX && (128 + CUT_Q + 2 * CUT_W) / 4 - CUT_Q >= NBMIN, "block split");

typedef unsigned short u16;
typedef unsigned long long u64;
typedef unsigned v4u __attribute__((ext_vector_type(4)));
typedef unsigned v2u __attribute__((ext_vector_type(2)));
typedef float f32x4 __attribute__((ext_vector_type(4)));
typedef float f32x2 __attribute__((ext_vector_type(2)));
typedef __bf16 bf16x8 __attribute__((ext_vector_type(8)));
typedef __bf16 bf16x2 __attribute__((ext_vector_type(2)));
typedef short s16x4 __attribute__((ext_vector_type(4)));
#define LAS __attribute__((address_space(3)))
#define DEV __device__ __forceinline__
typedef __amdgpu_buffer_rsrc_t rsrc_t;

constexpr int AUX_NT = 2, AUX_SC1 = 16;
constexpr int WS_EPOCH = 0, WS_TREF = 256, WS_READY = 2048, WS_STATS = 4096, WS_H1 = 12288, WS_H2 = WS_H1 + 42 * 2048;
constexpr int WS_H3 = WS_H2 + 8 * 4096 * 4, WS_H4 = WS_H3 + 262144, WS_H5 = WS_H4 + 458752, WS_H6 = WS_H5 + 8 * 14336;
constexpr int WS_END = WS_H6 + 8 * 28672;
constexpr int CLR_PER_WG = 4224;
constexpr int WS_CLR_END = WS_H3 + 256 * CLR_PER_WG;
static_assert(WS_END <= WS_CLR_END, "clear partition");
constexpr int WS_BYTES = WS_CLR_END;
constexpr int L_EW = 0;
constexpr int L_XN = 57344;
constexpr int L_RED = 65536;
constexpr int L_STK = L_RED + 16384;
constexpr int L_Q = L_STK + 256;
constexpr int L_P = L_Q + 2048;
constexpr int L_ML = L_P + 1280;
constexpr int L_FLAG = L_ML + 64;
constexpr int L_TCH = L_FLAG + 64;
constexpr int L_BYTES = L_TCH + 256;
enum { F_XN = 0, F_RED = 1, F_L = 2, F_SHADOW = 3, F_WLD = 4, F_P = 5, F_ML = 6, F_LATE = 7, F_PUB = 8, F_GO = 9, F_TREF = 10, F_T = 11 };

constexpr unsigned SPIN_G = 1u << 16, SPIN_L = 1u << 22;

struct Args { const u16* kc; unsigned char* ws; const u16 *x, *c, *wmod, *wq, *vc, *wo, *w1, *w3, *w2; u16* out; };
typedef unsigned v16u __attribute__((ext_vector_type(16)));
typedef unsigned v8u __attribute__((ext_vector_type(8)));
template <typename T> DEV T* mkptr(unsigned lo, unsigned hi) { return (T*)(__attribute__((address_space(1))) T*)(((u64)hi << 32) | lo); }
#define ASK_ARGS() \
  v16u ka; \
  asm volatile("s_load_dwordx16 %0, %1, 0x0" : "=&s"(ka) : "s"(__builtin_amdgcn_kernarg_segment_ptr()));
constexpr unsigned split_tab(int cut0, int cut2, bool first) {
  const int tot = 128 + cut0 + CUT_Q + cut2 + CUT_W, n0 = ((tot + 1) >> 2) - cut0, n1 = ((tot + 2) >> 2) - CUT_Q, n2 = ((tot + 3) >> 2) - cut2, n3 = 128 - n0 - n1 - n2;
  return first ? n0 << 8 | (n0 + n1) << 16 | (unsigned)(n0 + n1 + n2) << 24 : n0 | n1 << 8 | n2 << 16 | n3 << 24;
}
template <bool FIRST> DEV int split_of(int qd, int s) {
  const unsigned t = qd < 16 ? (qd < 14 ? split_tab(CUT_C, 0, FIRST) : split_tab(CUT_M, 0, FIRST)) : (qd < 42 ? split_tab(CUT_M, CUT_W, FIRST) : split_tab(CUT_O, CUT_W, FIRST));
  return (t >> (8 * s)) & 255;
}
#define SPLIT() \
  int qd_ = qd; asm volatile("" : "+s"(qd_)); \
  const int nb = split_of<false>(qd_, s), skip = NBMAX - nb;        \
  const unsigned kvb = (qd_ * 4096 + 3072 + split_of<true>(qd_, s) * 8) * 512; \
  const int rot = ((bid * 5 + r) & 7) - skip;
#define GET_ARGS() \
  asm volatile("s_waitcnt lgkmcnt(0)" : "+s"(ka)); \
  Args a = {mkptr<const u16>(ka[0], ka[1]), mkptr<unsigned char>(ka[2], ka[3]), mkptr<const u16>(ka[4], ka[5]), mkptr<const u16>(ka[6], ka[7]), \
            mkptr<const u16>(ka[8], ka[9]), mkptr<const u16>(ka[10], ka[11]), mkptr<const u16>(ka[12], ka[13]), mkptr<const u16>(ka[14], ka[15]), nullptr, nullptr, nullptr, nullptr}; \
  const rsrc_t rws = mkrsrc(a.ws, WS_BYTES); \
  const v4u aws = {ka[2], ka[3] & 0xffffu, (unsigned)WS_BYTES, 0x00020000u};       \
  const unsigned tstart = (unsigned)__builtin_amdgcn_s_memrealtime() - ALAG;

#define EXIT() __builtin_amdgcn_endpgm()
#define POLL_BEGIN(cap) { unsigned sp = (cap); bool ok; _Pragma("unroll 1") do { asm volatile("" ::: "memory");
#define POLL_END } while (!ok && --sp); asm volatile("" ::: "memory"); }
#define SB() __builtin_amdgcn_sched_barrier(0)

DEV rsrc_t mkrsrc(const void* p, unsigned bytes) { return __builtin_amdgcn_make_buffer_rsrc((void*)p, 0, bytes, 0x00020000); }
DEV f32x4 mfma(v4u a, v4u b, f32x4 c) { return __builtin_amdgcn_mfma_f32_16x16x32_bf16(__builtin_bit_cast(bf16x8, a), __builtin_bit_cast(bf16x8, b), c, 0, 0, 0); }
DEV unsigned pk(float lo, float hi) { f32x2 v = {lo, hi}; return __builtin_bit_cast(unsigned, __builtin_convertvector(v, bf16x2)); }
DEV float asf(unsigned u) { return __builtin_bit_cast(float, u); }
DEV unsigned asu(float f) { return __builtin_bit_cast(unsigned, f); }
DEV f32x4 asf4(v4u u) { return __builtin_bit_cast(f32x4, u); }
DEV float lo16(unsigned u) { return asf(u << 16); }
DEV float hi16(unsigned u) { return asf(u & 0xffff0000u); }
DEV float rb(float f) { return lo16(pk(f, f)); }
DEV unsigned hi2(unsigned lo, unsigned hi) { return __builtin_amdgcn_perm(hi, lo, 0x07060302u); }
DEV unsigned lo2(unsigned lo, unsigned hi) { return __builtin_amdgcn_perm(hi, lo, 0x05040100u); }
template <int CTRL> DEV unsigned dppu(unsigned x) { return (unsigned)__builtin_amdgcn_mov_dpp((int)x, CTRL, 0xf, 0xf, true); }
template <int CTRL> DEV float dpp(float x) { return asf(dppu<CTRL>(asu(x))); }
constexpr int ROR1 = 0x121, ROR2 = 0x122, ROR4 = 0x124, ROR8 = 0x128, ROR12 = 0x12C, HMIRROR = 0x141;
template <int BANKS> DEV unsigned half_from(unsigned old, unsigned src) { return (unsigned)__builtin_amdgcn_update_dpp((int)old, (int)src, ROR8, 0xf, BANKS, false); }
template <int BANKS> DEV unsigned mirror_in(unsigned v) { return (unsigned)__builtin_amdgcn_update_dpp((int)v, (int)v, HMIRROR, 0xf, BANKS, false); }
DEV float sum32swap(float a, float b) { auto r = __builtin_amdgcn_permlane32_swap(asu(a), asu(b), false, false); return asf(r[0]) + asf(r[1]); }
DEV float sum16swap(float a, float b) { auto r = __builtin_amdgcn_permlane16_swap(asu(a), asu(b), false, false); return asf(r[0]) + asf(r[1]); }
DEV float max32(float a) { auto r = __builtin_amdgcn_permlane32_swap(asu(a), asu(a), false, false); return fmaxf(asf(r[0]), asf(r[1])); }
DEV float rowsum(float v) { v += dpp<ROR8>(v); v += dpp<ROR4>(v); v += dpp<ROR2>(v); v += dpp<ROR1>(v); return v; }
DEV float rowmax(float v) { v = fmaxf(v, dpp<ROR8>(v)); v = fmaxf(v, dpp<ROR4>(v)); v = fmaxf(v, dpp<ROR2>(v)); v = fmaxf(v, dpp<ROR1>(v)); return v; }
DEV float wavesum(float v) { v = rowsum(v); v = sum16swap(v, v); return sum32swap(v, v); }
DEV unsigned umax3(unsigned a, unsigned b, unsigned c) { return max(max(a, b), c); }
DEV unsigned smax(unsigned acc, v4u v) { return umax3(umax3(acc, v.x, v.y), v.z, v.w); }
DEV unsigned tagx(unsigned acc, v4u v, unsigned tag) { return acc | (v.x ^ tag) | (v.y ^ tag) | (v.z ^ tag) | (v.w ^ tag); }
DEV unsigned nosent(unsigned v) { return min(v, 0xfffffffeu); }
DEV v4u nosent(v4u v) { return v4u{nosent(v.x), nosent(v.y), nosent(v.z), nosent(v.w)}; }
template <int N> DEV void waitvm() { asm volatile("s_waitcnt vmcnt(%0)" ::"i"(N) : "memory"); }
template <int T8, int T7, int T6> DEV void waitsteps(int x) {
    asm volatile("s_waitcnt vmcnt(7)\n\ts_cmp_lt_i32 %0, %1\n\ts_cbranch_scc1 1f\n\ts_waitcnt vmcnt(6)\n\ts_cmp_lt_i32 %0, %2\n\ts_cbranch_scc1 1f\n\t"
                 "s_waitcnt vmcnt(5)\n\ts_cmp_lt_i32 %0, %3\n\ts_cbranch_scc1 1f\n\ts_waitcnt vmcnt(4)\n1:"
                 ::"s"(x), "i"(T8), "i"(T7), "i"(T6) : "memory", "scc");
}
template <int R> DEV void waitpaced(int rem) { waitsteps<2 * R * TLAT / 15, 2 * R * TLAT / 13, 2 * R * TLAT / 11>(rem); }
DEV int now32() { return (int)__builtin_amdgcn_s_memrealtime(); }
template <int N, int END, typename F> DEV void sfor(F&& f) { if constexpr (N < END) { f(std::integral_constant<int, N>{}); sfor<N + 1, END>(f); } }
constexpr float LOG2E = 1.4426950408889634f;
DEV float ex2(float v) { return __builtin_amdgcn_exp2f(v); }
DEV float silu_p(float v) { return v / (1.f + ex2(v * -LOG2E)); }
DEV float silu(float v) { return v * __builtin_amdgcn_rcpf(1.f + ex2(v * -LOG2E)); }
constexpr f32x4 ZERO4 = {0.f, 0.f, 0.f, 0.f};

DEV v4u ldws(rsrc_t rs, int off) { return __builtin_amdgcn_raw_buffer_load_b128(rs, off, 0, AUX_SC1); }
DEV v2u ldws2(rsrc_t rs, int off) { return __builtin_amdgcn_raw_buffer_load_b64(rs, off, 0, AUX_SC1); }
DEV unsigned ldws1(rsrc_t rs, int off) { return __builtin_amdgcn_raw_buffer_load_b32(rs, off, 0, AUX_SC1); }
DEV void stws(rsrc_t rs, int off, v4u v) { __builtin_amdgcn_raw_buffer_store_b128(v, rs, off, 0, AUX_SC1); }
DEV void stws2(rsrc_t rs, int off, v2u v) { __builtin_amdgcn_raw_buffer_store_b64(v, rs, off, 0, AUX_SC1); }
DEV void dma16(const void* src, unsigned lds) {
  unsigned keep;
  asm volatile("s_mov_b32 %0, m0\n\ts_mov_b32 m0, %2\n\ts_nop 0\n\tglobal_load_lds_dwordx4 %1, off nt\n\ts_mov_b32 m0, %0" : "=&s"(keep) : "v"(src), "s"(lds) : "memory");
}

DEV void dma16_sc1(const void* src, unsigned lds) {
  unsigned keep;
  asm volatile("s_mov_b32 %0, m0\n\ts_mov_b32 m0, %2\n\ts_nop 0\n\tglobal_load_lds_dwordx4 %1, off sc1\n\ts_mov_b32 m0, %0" : "=&s"(keep) : "v"(src), "s"(lds) : "memory");
}

DEV void lds_signal(LAS unsigned char* L, int flag, int lane) {
  if (lane == 0) asm volatile("ds_add_u32 %0, %1" ::"v"(L_FLAG + 4 * flag), "v"(1u) : "memory");
}
DEV unsigned lds_peek(LAS unsigned char* L, int flag) {
  return __builtin_amdgcn_readfirstlane(__hip_atomic_load((LAS unsigned*)(L + L_FLAG) + flag, __ATOMIC_RELAXED, __HIP_MEMORY_SCOPE_WORKGROUP));
}
template <bool SLEEP = true> DEV void lds_wait(LAS unsigned char* L, int flag, unsigned want) {
  POLL_BEGIN(SPIN_L)
    ok = lds_peek(L, flag) >= want;
    if (SLEEP && !ok) __builtin_amdgcn_s_sleep(1);
  POLL_END
}
DEV v4u trfrag(LAS unsigned char* p0, LAS unsigned char* p1) {
  s16x4 a = __builtin_amdgcn_ds_read_tr16_b64_v4i16((LAS s16x4*)p0), b = __builtin_amdgcn_ds_read_tr16_b64_v4i16((LAS s16x4*)p1);
  v2u x = __builtin_bit_cast(v2u, a), y = __builtin_bit_cast(v2u, b);
  return v4u{x.x, x.y, y.x, y.y};
}
template <int RS> DEV v4u wfrag(LAS unsigned char* p, int lane) {
  p += (8 * (lane >> 4) + ((lane >> 2) & 3)) * RS + (lane & 3) * 8;
  return trfrag(p, p + 4 * RS);
}
DEV f32x4 red4(LAS unsigned char* red, int lane) {
  LAS f32x4* p = (LAS f32x4*)(red + lane * 16);
  return (p[0] + p[64]) + (p[128] + p[192]);
}
DEV int h1off(int b, int n) { return WS_H1 + (n >> 6) * 2048 + ((n >> 2) & 1) * 1024 + ((n >> 3) & 7) * 128 + b * 16 + (n & 3) * 4; }
DEV int colchunk(int m, int rho) { return 8 * (m >> 1) + 2 * (((rho & 1) << 1) | (rho >> 1)) + (m & 1); }
DEV void f_dma(const Args& a, int f, int m, int lane) {
  if (m >= 14) return;
  const unsigned char* wb = (const unsigned char*)((lane & 4) ? a.w3 : a.w1) + f * 64 + (lane & 3) * 16 + (lane >> 3) * 3584;
#pragma unroll
  for (int rho = 0; rho < 4; ++rho) dma16(wb + 8 * colchunk(m, rho) * 3584, L_EW + (4 * m + rho) * 1024);
}
DEV void touch(const void* src) {
  unsigned keep;
  asm volatile("s_mov_b32 %0, m0\n\ts_mov_b32 m0, %2\n\ts_nop 0\n\tglobal_load_lds_dword %1, off\n\ts_mov_b32 m0, %0" : "=&s"(keep) : "v"(src), "s"(L_TCH) : "memory");
}
DEV void o_dma(const Args& a, int j, int n, int lane) {
  dma16((const unsigned char*)a.w2 + j * 32 + (lane & 1) * 16 + (32 * n + (lane >> 1)) * 896, L_EW + n * 1024);
}

template <int N> DEV void gather_ask(v4u rs, const int (&off)[N], v4u (&d)[N]) {
#pragma unroll
  for (int i = 0; i < N; ++i) asm volatile("buffer_load_dwordx4 %0, %1, %2, 0 offen sc1" : "=v"(d[i]) : "v"(off[i]), "s"(rs) : "memory");
}
template <int N, bool TAGGED = false> DEV void gather_end(v4u rs, const int (&off)[N], v4u (&d)[N], unsigned tag = 0) {
  unsigned m[N], lim = ~0u;
  auto missing = [&]() {
    waitvm<0>();
    unsigned mx = 0;
#pragma unroll
    for (int i = 0; i < N; ++i) { asm volatile("" : "+v"(d[i])); m[i] = TAGGED ? ((tagx(0u, d[i], tag) & 0xffffu) ? ~0u : 0u) : smax(0u, d[i]); mx = max(mx, m[i]); }
    return min(mx, lim) == ~0u;
  };
#pragma unroll
  for (int i = 0; i < N; ++i) m[i] = 0u;
  unsigned sp = SPIN_G;
#pragma unroll 1
  do {
#pragma unroll
    for (int i = 0; i < N; ++i) {
      u64 sv;
      asm volatile("v_cmp_eq_u32 vcc, -1, %2\n\ts_and_saveexec_b64 %1, vcc\n\ts_cbranch_execz 1f\n\tbuffer_load_dwordx4 %0, %3, %4, 0 offen sc1\n1:\n\ts_mov_b64 exec, %1"
                   : "+v"(d[i]), "=&s"(sv) : "v"(m[i]), "v"(off[i]), "s"(rs) : "vcc", "scc", "memory");
    }
    lim = --sp ? ~0u : 0u;
    asm volatile("" : "+v"(lim));
  } while (__any(missing()));
}
template <int N> DEV void gather(v4u rs, const int (&off)[N], v4u (&d)[N]) { gather_ask<N>(rs, off, d); gather_end<N>(rs, off, d); }

DEV void shadow_wait(rsrc_t rws, unsigned e1, LAS unsigned char* L, int lane) {
  POLL_BEGIN(SPIN_G)
    const v4u t0 = ldws(rws, WS_READY + lane * 16), t1 = ldws(rws, WS_READY + 1024 + lane * 16);
    ok = __all(((t0.x ^ e1) | (t0.y ^ ~e1) | (t0.z ^ e1) | (t0.w ^ ~e1) | (t1.x ^ e1) | (t1.y ^ ~e1) | (t1.z ^ e1) | (t1.w ^ ~e1)) == 0u);
    if (!ok) __builtin_amdgcn_s_sleep(16);
  POLL_END
  lds_signal(L, F_P, lane);
}

DEV unsigned clear(rsrc_t rws, int bid, int lane) {
  const unsigned ep = ldws1(rws, WS_EPOCH);
  const v4u sent = {~0u, ~0u, ~0u, ~0u};
  const int co = WS_H3 + bid * CLR_PER_WG + lane * 16;
#pragma unroll
  for (int i = 0; i < 4; ++i) stws(rws, co + i * 1024, sent);
  if (lane < 8) stws(rws, co + 4096, sent);
  return ep;
}
DEV int origin(unsigned tref, unsigned own) {
  int d = (int)(tref - own);
  d = (unsigned)(d + 250) < 800u ? d : 0;
  d += (int)dppu<ROR4>(d); d += (int)dppu<ROR2>(d); d += (int)dppu<ROR1>(d);
  return (int)own + (__builtin_amdgcn_readfirstlane(d) >> 3);
}
DEV void announce(rsrc_t rws, int bid, int lane, unsigned ep) {
  waitvm<0>();
  const unsigned e1 = ep + 1u;
  if (lane == 0) stws2(rws, WS_READY + bid * 8, v2u{e1, ~e1});
}
#define LOCALS() \
  int bid = blockIdx.x, lane = threadIdx.x; \
  asm volatile("" : "+s"(bid)); asm volatile("" : "+v"(lane)); \
  lane &= 63; \
  const int xc = bid & 7, r = bid >> 3, kvh = xc, b = r & 7, s = r >> 3, qd = bid & 63; \
  const bool isQ = s == 1, isM = bid < 42, ptype = isM | isQ, crit = bid < 14; \
  const int col = lane & 15, g = lane >> 4, hb = col & 7, hf = col >> 3;

static __device__ void code_end();
__global__ void __launch_bounds__(512, 2) fused_block(Args) {
  extern __shared__ __attribute__((aligned(16))) unsigned char smem[];
  LAS unsigned char* L = (LAS unsigned char*)smem;
  const int w = __builtin_amdgcn_readfirstlane(threadIdx.x) >> 6;
  if (__builtin_expect(w < 4, 0)) {
    ASK_ARGS();
    LOCALS();
    const int vw = w;
    const int P1 = vw == 3 ? 3 : vw + 4;
    const int k0 = 64 * vw + 32 * hf + 8 * g, k1 = 64 * P1 + 32 * hf + 8 * g;
    const int c0 = qd * 64;
    const int l56 = lane < 56 ? lane : 55, stride = isM ? 5376 : 8192;
    int wo = c0 * 2 + (64 * vw + (lane >> 3)) * stride + (lane & 7) * 16;
    const int im = hb * 896 + k0 * 2, iq = 2 * vw * 896 + l56 * 16;
    int i0 = isM ? im : iq, i1 = i0 + (isM ? (P1 - vw) * 128 : 896), zero = 0;
    asm volatile("" : "+v"(i0), "+v"(i1), "+v"(zero), "+v"(wo));
    GET_ARGS();
    v4u in0, in1; unsigned ep;
    if (ptype) {
      asm volatile("global_load_dwordx4 %0, %3, %6\n\tglobal_load_dwordx4 %1, %4, %6\n\tglobal_load_dword %2, %5, %7 sc1"
                   : "=&v"(in0), "=&v"(in1), "=&v"(ep) : "v"(i0), "v"(i1), "v"(zero), "s"(isM ? a.c : a.x), "s"(a.ws + WS_EPOCH) : "memory");
      const u16* wb = isM ? a.wmod : a.wq;
#pragma unroll 1
      for (int i = 0, n = vw < 3 ? 16 : 8, ld = L_EW + 8192 * vw; i < n; ++i) {
        asm volatile("s_mov_b32 m0, %2\n\ts_nop 0\n\tglobal_load_lds_dwordx4 %0, %1 nt" ::"v"(wo), "s"(wb), "s"(ld) : "memory");
        if (!crit) { if (isQ) waitvm<QDEPTH - 1>(); else waitvm<EDEPTH - 1>(); }
        wo += (i == 7 ? 200 : 8) * stride; ld += i == 7 ? 25 * 1024 : 1024;
      }
    }
    if (vw == 0 && lane < 32) ((LAS unsigned*)(L + L_ML))[lane] = lane < 16 ? ~0u : 0u;
    asm volatile("s_waitcnt lgkmcnt(0)" ::: "memory");
    __builtin_amdgcn_s_barrier();
    if (ptype) {
      if (vw < 3) waitvm<16>(); else waitvm<8>();
      asm volatile("" : "+v"(in0), "+v"(in1), "+v"(ep)::"memory");
      unsigned tref;
      if (isQ) asm volatile("buffer_load_dword %0, %1, %2, 0 offen offset:%3 sc1" : "=v"(tref) : "v"(4 * hb), "s"(aws), "i"(WS_TREF) : "memory");
      const unsigned tag = (__builtin_amdgcn_readfirstlane(ep) + 1u) & 0xffffu;
      v4u act[2];
      if (isM) {
#pragma unroll
        for (int i = 0; i < 4; ++i) { act[0][i] = pk(silu_p(lo16(in0[i])), silu_p(hi16(in0[i]))); act[1][i] = pk(silu_p(lo16(in1[i])), silu_p(hi16(in1[i]))); }
      }
      if (isQ) {
#pragma unroll
        for (int rr = 0; rr < 2; ++rr) {
          const v4u in = rr ? in1 : in0;
          float t[8];
#pragma unroll
          for (int i = 0; i < 4; ++i) { t[2 * i] = lo16(in[i]); t[2 * i + 1] = hi16(in[i]); }
          float sm = 0.f;
#pragma unroll
          for (int i = 0; i < 8; ++i) sm += t[i];
          const float mean = wavesum(lane < 56 ? sm : 0.f) * (1.f / 448.f);
          float sq = 0.f;
#pragma unroll
          for (int i = 0; i < 8; ++i) { const float d = t[i] - mean; sq += d * d; }
          const float var = rb(wavesum(lane < 56 ? sq : 0.f) * (1.f / 448.f)), mb = rb(mean);
          const float sd = rb(sqrtf(rb(var + 1e-5f)));
          v4u o;
#pragma unroll
          for (int i = 0; i < 4; ++i) o[i] = pk(rb(t[2 * i] - mb) / sd, rb(t[2 * i + 1] - mb) / sd);
          if (lane < 56) *(LAS v4u*)(L + L_XN + ((2 * vw + rr) * 448 + lane * 8) * 2) = o;
        }
        lds_signal(L, F_XN, lane);
      }
      SB();
      v4u m[8];
      const int o0 = WS_H1 + vw * 2048 + hf * 512 + (g * 8 + hb) * 16, o1 = o0 + (P1 - vw) * 2048;
      const int mo[8] = {o0, o0 + 1024, o0 + 14336, o0 + 15360, o1, o1 + 1024, o1 + 14336, o1 + 15360};
      v4u wf[2][2][4];
#pragma unroll
      for (int pi = 0; pi < 2; ++pi)
#pragma unroll
        for (int h2 = 0; h2 < 2; ++h2) {
          if (pi == 0 && h2 == 0) { if (vw < 3) waitvm<8>(); else waitvm<0>(); }
          if (pi == 1 && h2 == 0) waitvm<4>();
          if (pi == 1 && h2 == 1) { waitvm<0>(); }
#pragma unroll
          for (int ct = 0; ct < 4; ++ct) wf[pi][h2][ct] = wfrag<128>(L + L_EW + (64 * (pi ? P1 : vw) + 32 * h2) * 128 + ct * 32, lane);
        }
      if (isQ) {
        lds_wait(L, F_XN, 4);
        const v4u xn0 = *(LAS v4u*)(L + L_XN + (hb * 448 + k0) * 2), xn1 = *(LAS v4u*)(L + L_XN + (hb * 448 + k1) * 2);
        asm volatile("" : "+v"(tref));
        const int tgo = origin(tref, tstart - VLAG) + GT0;
        for (int i = 0; i < 4096 && now32() - tgo < 0; ++i) __builtin_amdgcn_s_sleep(1);
        gather_ask<8>(aws, mo, m);
        gather_end<8, true>(aws, mo, m, tag);
#pragma unroll
        for (int pi = 0; pi < 2; ++pi) {
          const v4u xn = pi ? xn1 : xn0;
#pragma unroll
          for (int i = 0; i < 4; ++i) {
            const v4u sa = m[4 * pi + (i >> 1)], ca = m[4 * pi + 2 + (i >> 1)];
            const int j = 2 * (i & 1);
            const unsigned c2 = pk(1.f + hi16(ca[j]), 1.f + hi16(ca[j + 1]));
            const unsigned t2 = pk(lo16(xn[i]) * lo16(c2), hi16(xn[i]) * hi16(c2));
            act[pi][i] = pk(lo16(t2) + hi16(sa[j]), hi16(t2) + hi16(sa[j + 1]));
          }
        }
      }
      if (vw == 3) act[1] = v4u{0u, 0u, 0u, 0u};
#pragma unroll
      for (int t2 = 0; t2 < 2; ++t2) {
        const int ct = 2 * t2;
        f32x4 a0 = mfma(wf[0][0][ct], act[0], ZERO4), a1 = mfma(wf[0][0][ct + 1], act[0], ZERO4);
        f32x4 b0 = mfma(wf[0][1][ct], act[0], ZERO4), b1 = mfma(wf[0][1][ct + 1], act[0], ZERO4);
        a0 = mfma(wf[1][0][ct], act[1], a0); a1 = mfma(wf[1][0][ct + 1], act[1], a1);
        b0 = mfma(wf[1][1][ct], act[1], b0); b1 = mfma(wf[1][1][ct + 1], act[1], b1);
        f32x4 o;
#pragma unroll
        for (int i = 0; i < 4; ++i) o[i] = (hf ? b1[i] : a0[i]) + dpp<ROR8>(hf ? b0[i] : a1[i]);
        *(LAS f32x4*)(L + L_RED + t2 * 4096 + vw * 1024 + lane * 16) = o;
      }
      lds_signal(L, F_RED, lane);
      if (vw < 2) {
        lds_wait<false>(L, F_RED, 4);
        const f32x4 o = red4(L + L_RED + vw * 4096, lane);
        const unsigned p01 = pk(o[0], o[1]), p23 = pk(o[2], o[3]);
        const int n = c0 + 32 * vw + 16 * hf + 4 * g;
        stws(rws, isM ? h1off(hb, n) : WS_H2 + (hb * 4096 + n) * 4,
             v4u{(p01 << 16) | tag, (p01 & 0xffff0000u) | tag, (p23 << 16) | tag, (p23 & 0xffff0000u) | tag});
        if (crit) lds_signal(L, F_GO, lane);
      }
    }
    if (vw == 3 && (!ptype || crit)) announce(rws, bid, lane, clear(rws, bid, lane));
    SPLIT();
    auto blkoff = [&](int n) { int t = n + rot; asm volatile("" : "+s"(t)); return (t >= nb ? t - nb : t) * 4096; };
    v4u v[NBMAX], f[NBMAX / 8][8];
#pragma unroll
    for (int n = 0; n < NBMAX - NBMIN; ++n) { v[n] = v4u{0u, 0u, 0u, 0u}; asm volatile("" : "+v"(v[n])); }
    __builtin_amdgcn_s_barrier();
    const int tend = (int)lds_peek(L, F_TREF);
    const u64 vbase = (u64)a.vc + kvb;
    const v4u rv = {(unsigned)vbase, (unsigned)(vbase >> 32) & 0xffffu, (unsigned)nb * 4096u, 0x00020000u};
    const int vvo = (lane >> 3) * 512 + vw * 128 + (lane & 7) * 16;
    auto xpose = [&](int n) {
      asm volatile("" : "+v"(v[n]), "+v"(v[n + 1]));
#pragma unroll
      for (int j = 0; j < 8; ++j) f[n >> 3][j][(n & 7) >> 1] = (j & 1) ? hi2(v[n][j >> 1], v[n + 1][j >> 1]) : lo2(v[n][j >> 1], v[n + 1][j >> 1]);
    };
    auto xgroup = [&](int G) {
#pragma unroll
      for (int q = 0; q < 4; ++q) xpose(8 * G + 2 * q);
#pragma unroll
      for (int j = 0; j < 8; ++j) asm volatile("" : "+v"(f[G][j]));
    };
    if (r >= 18) {
      const unsigned char* wb = (const unsigned char*)(a.wo + (size_t)(2 * ((xc + 4) & 7) + (r >= 25)) * 256 * 448 + (r >= 25 ? r - 25 : r - 18) * 64) + (lane >> 3) * 896 + (lane & 7) * 16;
#pragma unroll
      for (int i = 0; i < 8; ++i) { dma16(wb + (8 * vw + i) * 8 * 896, L_EW + (8 * vw + i) * 1024); waitvm<DEPTH_B - 1>(); }
    }
    f32x4 acc[8];
#pragma unroll
    for (int j = 0; j < 8; ++j) acc[j] = ZERO4;
    int rem = tend - now32();
    sfor<0, NBMAX>([&](auto nc) {
      constexpr int n = nc, R = NBMAX - (n & ~1);
      if (n >= NBMAX - NBMIN || n >= skip) {
        int t;
        if (!(n & 1)) t = now32();
        if (n >= NBMAX - NBMIN) asm volatile("buffer_load_dwordx4 %0, %1, %2, %3 offen nt" : "=v"(v[n]) : "v"(vvo), "s"(rv), "s"(blkoff(n)) : "memory");
        else asm volatile("buffer_load_dwordx4 %0, %1, %2, %3 offen nt" : "+v"(v[n]) : "v"(vvo), "s"(rv), "s"(blkoff(n)) : "memory");
        if (!(n & 1)) rem = tend - t;
        if (vw == 0 && n + 1 == NBMAX) lds_signal(L, F_LATE, lane);
        if (n + 1 < NBMAX) waitpaced<R>(rem);
        if (n == NBMAX - NBMIN + 8) lds_signal(L, F_WLD, lane);
      }
      if constexpr (n == 33 || n == 35 || n == 37) {
        xgroup((n - 33) >> 1);
      }
    });
    __builtin_amdgcn_s_setprio(3);
    waitvm<8>();
#pragma unroll
    for (int q = 0; q < 4; ++q) xpose(24 + 2 * q);
#pragma unroll
    for (int j = 0; j < 8; ++j) asm volatile("" : "+v"(f[3][j]));
    lds_wait<false>(L, F_P, 5);
    v4u pf[5];
#pragma unroll
    for (int G = 0; G < 5; ++G) pf[G] = *(LAS v4u*)(L + L_P + ((G * 2 + ((col >> 2) & 1)) * 8 + 2 * g + (col & 1)) * 16);
    SB();
#pragma unroll
    for (int G = 0; G < 4; ++G)
#pragma unroll
      for (int j = 0; j < 8; ++j) acc[j] = mfma(pf[G], f[G][j], acc[j]);
    int pubo = (lane & 0x28) ? 1 << 30 : WS_H3 + (((2 * kvh + g) * 4 + vw) * 2 + (b >> 2)) * 2048 + (4 * (b & 3) + s) * 128 + hb * 16;
    asm volatile("" : "+v"(pubo));
    SB();
    waitvm<6>(); xpose(32); SB();
    waitvm<4>(); xpose(34); SB();
    waitvm<2>(); xpose(36); SB();
    waitvm<0>(); xpose(38);
#pragma unroll
    for (int j = 0; j < 8; ++j) acc[j] = mfma(pf[4], f[4][j], acc[j]);
    SB();
    float o[8];
#pragma unroll
    for (int j = 0; j < 8; ++j) { o[j] = acc[j][0] + dpp<ROR8>(acc[j][1]); asm volatile("" : "+v"(o[j])); }
    const v4u ov = nosent(v4u{pk(o[0], o[1]), pk(o[2], o[3]), pk(o[4], o[5]), pk(o[6], o[7])});
    stws(rws, pubo, ov);
    lds_signal(L, F_PUB, lane);
    EXIT();
  }

  ASK_ARGS();
  if (w == 7) {
    const u64 pc = __builtin_amdgcn_s_getpc();
    const int len = max((int)((u64)&code_end - pc), 0);
    const int o = min((int)((blockIdx.x & 0x38) * (16 * CODE_CH) + (threadIdx.x - 448) * 128), len);
    asm volatile("s_mov_b32 m0, %2\n\ts_nop 0\n\tglobal_load_lds_dword %0, %1" ::"v"(o), "s"(pc), "s"(L_TCH));
  }
  LOCALS();
  const int kw = w - 4;
  SPLIT();
  int vo = ((lane >> 5) * 4 + ((lane >> 2) & 3)) * 512 + (((lane >> 4) & 1) * 4 + (lane & 3)) * 16, kbytes = nb * 4096, kvb_ = kvb, rotk = rot + kw, skipk = skip - kw;
  asm volatile("" : "+v"(vo));
  asm volatile("" : "+s"(kbytes), "+s"(kvb_), "+s"(rotk), "+s"(skipk));
  GET_ARGS();
  unsigned tag, ep = 0;
  {
    const rsrc_t rk = mkrsrc((const unsigned char*)a.kc + (unsigned)kvb_, kbytes);
    v4u k[NBMAX];
    unsigned tref = tstart;
    int tend = (int)tstart + TEND;
    const int after = nb + (r >= 18 ? 8 : 0);
    if (__builtin_expect(ptype, 0)) {
      const bool mine = !crit && kw == 2;
      if (mine) ep = clear(rws, bid, lane);
      __builtin_amdgcn_s_barrier();
      if (mine) announce(rws, bid, lane, ep);
      if (crit) lds_wait(L, F_GO, 2);
    }
#pragma unroll
    for (int i = 0; i < NBMAX / 4; ++i) {
      if (4 * i >= NBMAX - NBMIN || 4 * i >= skipk) {
        int tk = 4 * i + rotk;
        asm volatile("" : "+s"(tk));
        const int so = (tk >= nb ? tk - nb : tk) * 4096;
        int step = 0;
        if (i >= PACE_K) {
          const int rem = tend - now32(), need = 2 * TLATK * (NBMAX - 4 * i + after);
          step = __builtin_amdgcn_readfirstlane(need > 13 * rem ? (need > 15 * rem ? 0 : 1) : need > 11 * rem ? 2 : 3);
        }
#pragma unroll
        for (int j = 0; j < 4; ++j) {
          k[4 * i + j] = __builtin_amdgcn_raw_buffer_load_b128(rk, vo + j * 128, so, AUX_NT);
          SB();
          if (4 * i + j + 1 < NBMAX) { if (i >= PACE_K) waitsteps<1, 2, 3>(step); else waitvm<DEPTH_A - 1>(); }
        }
      }
      if (i == NBMAX / 4 - 3) { ep = ldws1(rws, WS_EPOCH); SB(); }
      if (i == TREF_AT) { tref = ldws1(rws, WS_TREF + 4 * (lane & 7)); SB(); }
      if (i == TREF_AT + 2) { tend = origin(tref, tstart) + TEND; SB(); }
      if (i == 1 && !ptype) __builtin_amdgcn_s_barrier();
      if (i == 2 && bid >= 248 && kw == 3) { const int t = now32() - TOFF; if (lane == 0) __builtin_amdgcn_raw_buffer_store_b32(t, rws, WS_TREF + 4 * xc, 0, AUX_SC1); }
    }
    tag = (__builtin_amdgcn_readfirstlane(ep) + 1u) & 0xffffu;
    if (kw < 2) *(LAS v4u*)(L + L_Q + kw * 1024 + lane * 16) = v4u{~tag, ~tag, ~tag, ~tag};
    if (kw == 3 && lane == 0) ((LAS int*)(L + L_FLAG))[F_TREF] = tend;
    asm volatile("s_waitcnt lgkmcnt(0)" ::: "memory");
    __builtin_amdgcn_s_barrier();
    __builtin_amdgcn_s_setprio(2);
    const unsigned char* qsrc = a.ws + WS_H2 + (b * 4096 + kvh * 512) * 4 + lane * 16;
    unsigned qnext = tend + (QT0 - TEND);
    auto ask_q = [&]() {
      const unsigned now = (unsigned)__builtin_amdgcn_s_memrealtime();
      if ((int)(now - qnext) >= 0) { dma16_sc1(qsrc, L_Q); dma16_sc1(qsrc + 1024, L_Q + 1024); qnext = now + QDT; }
    };
    if (kw == 0) ask_q();
    if (kw == 1) { shadow_wait(rws, ep + 1u, L, lane); }
    __builtin_amdgcn_s_waitcnt(0x0F70);
    v4u qf[4];
    {
      v4u qt[8];
      POLL_BEGIN(SPIN_L)
        unsigned t = 0;
#pragma unroll
        for (int j = 0; j < 8; ++j) { qt[j] = *(volatile LAS v4u*)(L + L_Q + (((col >> 2) & 1) * 256 + 64 * (j >> 1) + 8 * (4 * (g & 1) + (col & 3))) * 4 + (j & 1) * 16); t = tagx(t, qt[j], tag); }
        ok = __all((t & 0xffffu) == 0u);
        if (!ok) {
          if (kw == 0) ask_q();
          __builtin_amdgcn_s_sleep(1);
        }
      POLL_END
      const unsigned keep = (g >> 1) == hf ? ~0u : 0u;
#pragma unroll
      for (int j = 0; j < 4; ++j) qf[j] = v4u{hi2(qt[2 * j].x, qt[2 * j].y) & keep, hi2(qt[2 * j].z, qt[2 * j].w) & keep, hi2(qt[2 * j + 1].x, qt[2 * j + 1].y) & keep, hi2(qt[2 * j + 1].z, qt[2 * j + 1].w) & keep};
    }
    float S[3];
    const int bb = lane & 3;
#pragma unroll
    for (int i = 0; i < 10; ++i) {
      f32x4 d = ZERO4;
#pragma unroll
      for (int j = 0; j < 4; ++j) d = mfma(qf[j], k[4 * i + j], d);
      const float sc = (dpp<0x00>(d[0]) + dpp<0x55>(d[1])) + (dpp<0xAA>(d[2]) + dpp<0xFF>(d[3]));
      if ((i & 3) == 0) S[i >> 2] = sc; else S[i >> 2] = bb == (i & 3) ? sc : S[i >> 2];
    }
#pragma unroll
    for (int u = 0; u < 3; ++u) S[u] = (unsigned)(16 * u + 4 * bb + kw - skip) < (unsigned)nb ? rb(S[u]) * 0.0625f : -INFINITY;
    const int hh = g & 1;
    const float mw = max32(rowmax(fmaxf(fmaxf(S[0], S[1]), S[2])));
    if ((lane & 0x2f) == 0) *(volatile LAS unsigned*)(L + L_ML + (hh * 4 + kw) * 4) = nosent(asu(mw));
    f32x4 m4;
    POLL_BEGIN(SPIN_L)
      m4 = *(volatile LAS f32x4*)(L + L_ML + hh * 16);
      ok = __all(smax(0u, __builtin_bit_cast(v4u, m4)) != ~0u);
    POLL_END
    const float m = fmaxf(fmaxf(m4[0], m4[1]), fmaxf(m4[2], m4[3]));
    const float e0 = ex2((S[0] - m) * LOG2E), e1 = ex2((S[1] - m) * LOG2E), e2 = ex2((S[2] - m) * LOG2E);
    {
      const unsigned pp = pk(e0, e1), p2 = pk(e2, e2);
      LAS unsigned char* pb = L + L_P + (((bb >> 1) * 2 + hh) * 8 + 4 * (g >> 1) + (col >> 2)) * 16 + (4 * (bb & 1) + kw) * 2;
      *(LAS u16*)pb = (u16)pp;
      *(LAS u16*)(pb + 512) = (u16)(pp >> 16);
      if (bb < 2) *(LAS u16*)(pb + 1024) = (u16)p2;
    }
    lds_signal(L, F_P, lane);
    float lw = rowsum((e0 + e1) + e2);
    lw = sum32swap(lw, lw);
    if ((lane & 0x2f) == 0) *(LAS float*)(L + L_ML + 32 + (hh * 4 + kw) * 4) = lw;
    lds_signal(L, F_L, lane);
    if (kw == 0) {
      lds_wait(L, F_L, 4);
      const f32x4 l4 = *(LAS f32x4*)(L + L_ML + 32 + hh * 16);
      if ((lane & 0x2f) == 0) stws(rws, WS_STATS + (b * 8 + kvh) * 128 + s * 32 + hh * 16, v4u{asu((l4[0] + l4[1]) + (l4[2] + l4[3])), asu(m), ep + 1u, ~(ep + 1u)});
    }
    __builtin_amdgcn_s_setprio(0);
  }

  if (r >= 18) {
    const int h = 2 * ((xc + 4) & 7) + (r >= 25), cb64 = r >= 25 ? r - 25 : r - 18, khalf = kw >> 1, rt = kw & 1;
    const int tb = xc, m = r - 18;
    const int rcol = colchunk(m, g) * 8 + hb;
    const float xv = lo16(a.x[tb * 448 + rcol]);
    lds_wait(L, F_WLD, 4);
    v4u wf[4][4];
#pragma unroll
    for (int i = 0; i < 4; ++i)
#pragma unroll
      for (int ct = 0; ct < 4; ++ct) { wf[i][ct] = wfrag<128>(L + L_EW + (128 * khalf + 32 * i) * 128 + ct * 32, lane); asm volatile("" : "+v"(wf[i][ct])); }
    lds_wait(L, F_LATE, 1);
    unsigned ga, cb;
    v4u pe[4];
    auto stats = [&]() {
      ga = ldws1(rws, h1off(tb, 2 * 448 + rcol)); cb = ldws1(rws, h1off(tb, 4 * 448 + rcol));
      unsigned t2 = ((ga ^ tag) | (cb ^ tag)) & 0xffffu;
#pragma unroll
      for (int i = 0; i < 4; ++i) { pe[i] = ldws(rws, WS_STATS + ((4 * rt + g) * 8 + (h >> 1)) * 128 + i * 32 + (h & 1) * 16); t2 |= (pe[i].z ^ (ep + 1u)) | (pe[i].w ^ ~(ep + 1u)); }
      return __all(t2 == 0u);
    };
    const bool have = stats();
    const int o0 = WS_H3 + ((h * 4 + 2 * khalf) * 2 + rt) * 2048 + hb * 128 + hf * 64 + g * 16;
    const int oo[4] = {o0, o0 + 1024, o0 + 4096, o0 + 5120};
    const int yo = WS_H4 + (((4 * rt + g) * 14 + 2 * cb64 + hf) * 32 + 2 * h + khalf) * 128 + hb * 16;
    asm volatile("" ::"v"(yo), "v"(oo[0]), "v"(oo[1]), "v"(oo[2]), "v"(oo[3]), "v"(xv));
    lds_wait<false>(L, F_PUB, 4);
    v4u of[4];
    gather_ask<4>(aws, oo, of);
    if (!have)
      POLL_BEGIN(SPIN_G)
        ok = stats();
      POLL_END
    float wgt[4];
    const float M = fmaxf(fmaxf(asf(pe[0].y), asf(pe[1].y)), fmaxf(asf(pe[2].y), asf(pe[3].y)));
#pragma unroll
    for (int i = 0; i < 4; ++i) wgt[i] = ex2((asf(pe[i].y) - M) * LOG2E);
    const float iL = 1.f / ((wgt[0] * asf(pe[0].x) + wgt[1] * asf(pe[1].x)) + (wgt[2] * asf(pe[2].x) + wgt[3] * asf(pe[3].x)));
#pragma unroll
    for (int i = 0; i < 4; ++i) wgt[i] *= iL;
    const float gaf = hi16(ga), cc = hf ? 1.f : rb(1.f + hi16(cb));
    f32x2 w01 = {wgt[0], wgt[1]}, w23 = {wgt[2], wgt[3]};
    asm volatile("" : "+v"(w01), "+v"(w23) : "v"(gaf), "v"(cc));
    gather_end<4>(aws, oo, of);
    {
      f32x4 d[4] = {ZERO4, ZERO4, ZERO4, ZERO4};
#pragma unroll
      for (int i = 0; i < 4; ++i) {
        v4u fr;
#pragma unroll
        for (int e = 0; e < 4; ++e) fr[e] = (i & 1) ? half_from<0x3>(of[i][e], of[i - 1][e]) : half_from<0xC>(of[i][e], of[i + 1][e]);
#pragma unroll
        for (int ct = 0; ct < 4; ++ct) d[ct] = mfma(fr, wf[i][ct], d[ct]);
      }
      SB();
      v4u o;
#pragma unroll
      for (int ct = 0; ct < 4; ++ct) {
        f32x2 t = __builtin_elementwise_fma(d[ct].hi, w23, d[ct].lo * w01);
        asm volatile("" : "+v"(t));
        o[ct] = nosent(asu(t[0] + t[1]));
      }
      stws(rws, yo, o);
    }
    if (kw != 0) { EXIT(); }
    v4u y[4];
    const int y0 = WS_H4 + (tb * 14 + m) * 4096 + lane * 16;
    int zo = WS_H5 + hb * 14336 + (m * 8 + tb) * 128 + hf * 64 + g * 16;
    asm volatile("" : "+v"(zo));
    __builtin_amdgcn_s_sleep(RB_DELAY);
    gather<4>(aws, {y0, y0 + 1024, y0 + 2048, y0 + 3072}, y);
    const f32x4 sv = (asf4(y[0]) + asf4(y[1])) + (asf4(y[2]) + asf4(y[3]));
    float u = sum16swap(sum32swap(sv[0], sv[1]), sum32swap(sv[2], sv[3]));
    u += dpp<ROR8>(u);
    const float x1 = rb(xv + rb(gaf * rb(u))), val = x1 * cc;
    const unsigned d1 = nosent(pk(dpp<0xA0>(val), dpp<0xF5>(val)));
    const unsigned e0 = dppu<0x00>(d1), e1 = dppu<0xAA>(d1);
    stws(rws, zo, v4u{mirror_in<0xA>(e0), mirror_in<0xA>(e1), mirror_in<0x5>(e0), mirror_in<0x5>(e1)});
    EXIT();
  }

  if (xc == 7) { waitvm<0>(); EXIT(); }
  v8u kb;
  asm volatile("s_load_dwordx8 %0, %1, 0x40\n\ts_waitcnt lgkmcnt(0)" : "=&s"(kb) : "s"(__builtin_amdgcn_kernarg_segment_ptr()));
  a.w1 = mkptr<const u16>(kb[0], kb[1]); a.w3 = mkptr<const u16>(kb[2], kb[3]); a.w2 = mkptr<const u16>(kb[4], kb[5]); a.out = mkptr<u16>(kb[6], kb[7]);
  auto warm = [=]() __attribute__((always_inline)) {
    const int sl = (r < 8 ? r : r - 8) * 4 + kw;
#pragma unroll
    for (int i = 0; i < 2; ++i) {
      const int t = (i * 40 + sl) * 64 + lane;
      if (i * 40 + sl < 56) touch((const unsigned char*)(t < 1792 ? a.w1 : a.w3 - 448 * 1792) + (t >> 2) * 3584 + xc * 512 + (t & 3) * 128);
    }
    __builtin_amdgcn_s_sleep(TSPLIT);
    if (sl >= 12) touch((const unsigned char*)a.w2 + ((sl - 12) * 64 + lane) * 896 + xc * 128);
  };
  if (s == 2) { lds_wait(L, F_PUB, 4); warm(); waitvm<0>(); EXIT(); }

  if (isQ) {
    const int f = 8 * xc + b;
    v4u cs[4];
    {
      v4u m[8];
      POLL_BEGIN(SPIN_G)
        unsigned t = 0;
#pragma unroll
        for (int i = 0; i < 4; ++i) {
          const int o = h1off(hb, (hf ? 3 : 4) * 448 + 8 * colchunk(min(kw + 4 * i, 13), g));
          m[2 * i] = ldws(rws, o); m[2 * i + 1] = ldws(rws, o + 1024);
        }
#pragma unroll
        for (int i = 0; i < 8; ++i) t = tagx(t, m[i], tag);
        ok = __all((t & 0xffffu) == 0u);
        if (!ok) __builtin_amdgcn_s_sleep(4);
      POLL_END
      const float one = hf ? 0.f : 1.f;
#pragma unroll
      for (int i = 0; i < 4; ++i)
#pragma unroll
        for (int e = 0; e < 4; ++e) cs[i][e] = pk(one + hi16(m[2 * i + (e >> 1)][2 * (e & 1)]), one + hi16(m[2 * i + (e >> 1)][2 * (e & 1) + 1]));
    }
    lds_wait(L, F_PUB, 4);
#pragma unroll
    for (int i = 0; i < 4; ++i) f_dma(a, f, kw + 4 * i, lane);
    v4u wf[4][4];
    {
      f32x4 t[4] = {ZERO4, ZERO4, ZERO4, ZERO4};
#pragma unroll
      for (int i = 0; i < 4; ++i) {
        if (i == 0) { if (kw < 2) waitvm<12>(); else waitvm<8>(); }
        if (i == 1) { if (kw < 2) waitvm<8>(); else waitvm<4>(); }
        if (i == 2) { if (kw < 2) waitvm<4>(); else waitvm<0>(); }
        if (i == 3) { waitvm<0>(); }
#pragma unroll
        for (int ct = 0; ct < 4; ++ct) {
          wf[i][ct] = wfrag<128>(L + L_EW + (i == 3 && kw >= 2 ? kw : kw + 4 * i) * 4096 + ct * 32, lane);
          if (i == 3 && kw >= 2) wf[i][ct] = v4u{0u, 0u, 0u, 0u};
          t[ct] = mfma(wf[i][ct], cs[i], t[ct]);
        }
#pragma unroll
        for (int ct = 0; ct < 4; ++ct) asm volatile("" : "+v"(t[ct]));
      }
#pragma unroll
      for (int ct = 0; ct < 4; ++ct) *(LAS f32x4*)(L + L_RED + ct * 4096 + kw * 1024 + lane * 16) = t[ct];
    }
    lds_signal(L, F_T, lane);
    v4u zz[4];
    const int z0 = WS_H5 + xc * 14336 + kw * 1024 + hb * 128 + hf * 64 + g * 16;
    const int zo[4] = {z0, z0 + 4096, z0 + 8192, z0 + (kw < 2 ? 12288 : 8192)};
    gather_ask<4>(aws, zo, zz);
    lds_wait<false>(L, F_T, 4);
    f32x2 T[2][2];
#pragma unroll
    for (int u = 0; u < 2; ++u)
#pragma unroll
      for (int t = 0; t < 2; ++t) {
        LAS f32x2* p = (LAS f32x2*)(L + L_RED + (2 * u + (kw & 1)) * 4096 + (16 * g + 8 * t + hb) * 16 + hf * 8);
        T[u][t] = (p[0] + p[128]) + (p[256] + p[384]);
        asm volatile("" : "+v"(T[u][t]));
      }
    const int junk = L_EW + kw * 4096 + lane * 16;
    int zw = hf ? junk : L_XN + kw * 512 + (8 * g + hb) * 16, zstep = hf ? 0 : 2048;
    int sw = hf && (col >> 2) == g ? L_STK + (hb * 4 + kw) * 8 : junk + 1024, sr = L_STK + hb * 32, zr = L_XN + (kw & 1) * 2048 + (8 * g + hb) * 16 + hf * 8;
    int zw1 = zw + zstep, zw2 = zw1 + zstep, zw3 = zw2 + zstep;
    int ho0 = WS_H6 + (2 * (kw >> 1) + hf) * 28672 + (2 * f + (kw & 1)) * 256 + hb * 32 + g * 8, ho1 = ho0 + 4 * 28672;
    unsigned last = kw < 2 ? ~0u : 0u, one2 = 0x3f803f80u;
    v4u dm = {(col & 3) == 0 ? ~0u : 0u, (col & 3) == 1 ? ~0u : 0u, (col & 3) == 2 ? ~0u : 0u, (col & 3) == 3 ? ~0u : 0u}, ones = {one2, one2, one2, one2};
    asm volatile("" : "+v"(zw), "+v"(zw1), "+v"(zw2), "+v"(zw3), "+v"(sw), "+v"(sr), "+v"(zr), "+v"(ho0), "+v"(ho1), "+v"(last), "+v"(dm), "+v"(ones));
    gather_end<4>(aws, zo, zz);
    {
      zz[3] &= last;
      f32x4 s1 = ZERO4, s2 = ZERO4, ac[4] = {ZERO4, ZERO4, ZERO4, ZERO4};
#pragma unroll
      for (int i = 0; i < 4; ++i) {
        s1 = mfma(ones, zz[i], s1); s2 = mfma(zz[i], zz[i], s2);
#pragma unroll
        for (int ct = 0; ct < 4; ++ct) ac[ct] = mfma(wf[i][ct], zz[i], ac[ct]);
        SB();
      }
      const unsigned dg = ((asu(s2[0]) & dm[0]) | (asu(s2[1]) & dm[1])) | ((asu(s2[2]) & dm[2]) | (asu(s2[3]) & dm[3]));
      *(LAS f32x4*)zw = ac[0]; *(LAS f32x4*)zw1 = ac[1]; *(LAS f32x4*)zw2 = ac[2]; *(LAS f32x4*)zw3 = ac[3];
      *(LAS f32x2*)sw = f32x2{s1[0], asf(dg)};
    }
    asm volatile("s_waitcnt lgkmcnt(0)" ::: "memory");
    __builtin_amdgcn_s_barrier();
    const f32x4 ta = ((LAS f32x4*)sr)[0], tb = ((LAS f32x4*)sr)[1];
    f32x2 zp[8];
#pragma unroll
    for (int i = 0; i < 8; ++i) zp[i] = ((LAS f32x2*)zr)[(i >> 2) * 512 + (i & 3) * 64];
    SB();
    const f32x4 t0 = ta + tb;
    const f32x2 Zg = (zp[0] + zp[1]) + (zp[2] + zp[3]), Zu = (zp[4] + zp[5]) + (zp[6] + zp[7]);
    const float mu = (t0[0] + t0[2]) * (1.f / 448.f), var = fmaxf((t0[1] + t0[3]) * (1.f / 448.f) - mu * mu, 0.f);
    const float rs = __builtin_amdgcn_rsqf(var + 1e-5f);
    const f32x2 nmu = {-mu, -mu}, rs2 = {rs, rs};
    const f32x2 vg = __builtin_elementwise_fma(rs2, __builtin_elementwise_fma(nmu, T[0][0], Zg), T[0][1]), vu = __builtin_elementwise_fma(rs2, __builtin_elementwise_fma(nmu, T[1][0], Zu), T[1][1]);
    const unsigned gp = pk(vg[0], vg[1]), up = pk(vu[0], vu[1]);
    const unsigned sp2 = pk(silu(lo16(gp)), silu(hi16(gp)));
    const unsigned hp = nosent(pk(lo16(sp2) * lo16(up), hi16(sp2) * hi16(up)));
    const v2u hv = {half_from<0xC>(hp, hp), half_from<0x3>(hp, hp)};
    stws2(rws, ho0, hv); stws2(rws, ho1, hv);
    EXIT();
  }

  {
    const int j = 4 * xc + (b & 3), row = (b & 4) + (col & 3);
    const int oc = 16 * j + 4 * g;
    v4u gb;
    POLL_BEGIN(SPIN_G)
      gb = ldws(rws, h1off(row, 5 * 448 + oc));
      ok = __all((tagx(0u, gb, tag) & 0xffffu) == 0u);
      if (!ok) __builtin_amdgcn_s_sleep(4);
    POLL_END
    lds_wait(L, F_PUB, 4);
    warm();
    __builtin_amdgcn_s_sleep(O_DELAY);
#pragma unroll
    for (int i = 0; i < 16; ++i) if (i < 12 || kw < 2) o_dma(a, j, 4 * kw + 16 * (i >> 2) + (i & 3), lane);
    waitvm<0>();
    v4u wf[4][4];
#pragma unroll
    for (int i = 0; i < 4; ++i)
#pragma unroll
      for (int q = 0; q < 4; ++q) {
        wf[i][q] = wfrag<32>(L + L_EW + (4 * kw + (i == 3 && kw >= 2 ? 0 : 16 * i) + q) * 1024, lane);
        if (i == 3 && kw >= 2) wf[i][q] = v4u{0u, 0u, 0u, 0u};
        asm volatile("" : "+v"(wf[i][q]));
      }
    v2u xz;
    {
      const int c8 = oc >> 3, mm = 2 * (c8 >> 3) + (c8 & 1), ct2 = (c8 >> 1) & 3, rh = ((ct2 & 1) << 1) | (ct2 >> 1);
      POLL_BEGIN(SPIN_G)
        xz = ldws2(rws, WS_H5 + xc * 14336 + (mm * 8 + row) * 128 + 64 + rh * 16 + (oc & 4) * 2);
        ok = __all(max(xz.x, xz.y) != ~0u);
        if (!ok) __builtin_amdgcn_s_sleep(2);
      POLL_END
    }
    f32x4 gbf = {hi16(gb.x), hi16(gb.y), hi16(gb.z), hi16(gb.w)}, x1f = {lo16(xz.x), hi16(xz.x), lo16(xz.y), hi16(xz.y)};
    int oo = col < 4 ? (row * 448 + oc) * 2 : 1 << 20, rw = L_RED + kw * 1024 + lane * 16, rr = L_RED + lane * 16;
    v4u ro = {kb[6], kb[7] & 0xffffu, 8 * 448 * 2, 0x00020000u};
    asm volatile("" : "+v"(gbf), "+v"(x1f), "+v"(oo), "+v"(rw), "+v"(rr));
    asm volatile("" : "+s"(ro));
    v4u hh[4];
    const int h0 = WS_H6 + xc * 28672 + kw * 2048 + (2 * (col >> 2) + (g >> 1)) * 256 + row * 32 + (g & 1) * 16;
    gather<4>(aws, {h0, h0 + 8192, h0 + 16384, h0 + (kw < 2 ? 24576 : 16384)}, hh);
    {
      f32x4 ac[4] = {ZERO4, ZERO4, ZERO4, ZERO4};
#pragma unroll
      for (int i = 0; i < 4; ++i)
#pragma unroll
        for (int q = 0; q < 4; ++q) ac[q] = mfma(wf[i][q], hh[i], ac[q]);
      SB();
      f32x4 o;
#pragma unroll
      for (int i = 0; i < 4; ++i) {
        float t = ac[0][i] + dpp<ROR12>(ac[1][i]), u = ac[2][i] + dpp<ROR12>(ac[3][i]);
        asm volatile("" : "+v"(t), "+v"(u));
        t += dpp<ROR8>(u);
        asm volatile("" : "+v"(t));
        o[i] = t;
      }
      *(LAS f32x4*)rw = o;
    }
    asm volatile("s_waitcnt lgkmcnt(0)" ::: "memory");
    __builtin_amdgcn_s_barrier();
    if (kw == 0) {
      const f32x4 Y = red4((LAS unsigned char*)rr, 0);
      const unsigned y01 = pk(Y[0], Y[1]), y23 = pk(Y[2], Y[3]);
      const unsigned t01 = pk(gbf[0] * lo16(y01), gbf[1] * hi16(y01)), t23 = pk(gbf[2] * lo16(y23), gbf[3] * hi16(y23));
      const v2u o = {pk(x1f[0] + lo16(t01), x1f[1] + hi16(t01)), pk(x1f[2] + lo16(t23), x1f[3] + hi16(t23))};
      asm volatile("buffer_store_dwordx2 %0, %1, %2, 0 offen" ::"v"(o), "v"(oo), "s"(ro) : "memory");
      if (bid == 0 && lane == 0) __builtin_amdgcn_raw_buffer_store_b32(ep + 1u, rws, WS_EPOCH, 0, AUX_SC1);
    }
  }
}

static __device__ __attribute__((noinline, used)) void code_end() {}

extern "C" void kernel_launch(void* const* d_in, const int* in_sizes, int n_in, void* d_out, int out_size, void* d_ws, size_t ws_size, hipStream_t stream) {
  Args a;
  a.x = (const u16*)d_in[0]; a.kc = (const u16*)d_in[1]; a.vc = (const u16*)d_in[2]; a.wq = (const u16*)d_in[3]; a.wo = (const u16*)d_in[4];
  a.w1 = (const u16*)d_in[6]; a.w3 = (const u16*)d_in[7]; a.w2 = (const u16*)d_in[8]; a.c = (const u16*)d_in[9]; a.wmod = (const u16*)d_in[10];
  a.out = (u16*)d_out; a.ws = (unsigned char*)d_ws;
  (void)hipFuncSetAttribute((const void*)fused_block, hipFuncAttributeMaxDynamicSharedMemorySize, L_BYTES);
  fused_block<<<256, 512, L_BYTES, stream>>>(a);
}
```
